```python
import torch

def _softmax(x, axis=-1):
    return torch.softmax(x, dim=axis)

def _gelu(x):
    return torch.nn.functional.gelu(x, approximate='tanh')

def _silu(x):
    return torch.nn.functional.silu(x)

def _sigmoid(x):
    return torch.sigmoid(x)

def _layernorm(x, axis=-1, eps=1e-05):
    m = x.mean(dim=axis, keepdim=True)
    v = x.var(dim=axis, keepdim=True, unbiased=False)
    return (x - m) / torch.sqrt(v + eps)

def _scatter_add(tgt, idx, src, axis=0):
    return tgt.clone().index_add_(axis, idx, src)

def _segment_sum(src, seg, n):
    out = torch.zeros((n,) + src.shape[1:], dtype=src.dtype)
    return out.index_add_(0, seg, src)

def _pad(x, pw):
    flat = tuple((v for pair in reversed(pw) for v in pair))
    return torch.nn.functional.pad(x, flat)
kernel_name = 'parallel_sum__ssm_conv_s4__mixer_gmlp__prompt'

def setup_inputs():

    _w = lambda a: (lambda t: t.to(torch.bfloat16) if t.is_floating_point() else t)(torch.as_tensor(a))

    def _A_setup():
        B, L, D, N = (1, 2048, 512, 128)
        A_re = -(lambda _x: torch.exp(torch.as_tensor(_x)))((lambda *s: torch.randn(*s, dtype=torch.bfloat16) * 3.2)(D, N))
        A_im = torch.pi * (lambda *s: torch.randn(*s, dtype=torch.bfloat16) * 0.1)(D, N)
        B_re = (lambda *s: torch.randn(*s, dtype=torch.bfloat16) * 0.8)(D, N)
        B_im = (lambda *s: torch.randn(*s, dtype=torch.bfloat16) * 0.8)(D, N)
        C_re = (lambda *s: torch.randn(*s, dtype=torch.bfloat16) * 0.8)(D, N)
        C_im = (lambda *s: torch.randn(*s, dtype=torch.bfloat16) * 0.8)(D, N)
        log_dt = (lambda *s: torch.randn(*s, dtype=torch.bfloat16) * 0.1)(D) * 0.5 - 4.0
        Dskip = (lambda *s: torch.randn(*s, dtype=torch.bfloat16) * 0.8)(D)
        return {'u': _w((lambda *s: torch.randn(*s, dtype=torch.bfloat16) * 0.05)(B, L, D)), 'A_re': _w(A_re), 'A_im': _w(A_im), 'B_re': _w(B_re), 'B_im': _w(B_im), 'C_re': _w(C_re), 'C_im': _w(C_im), 'log_dt': _w(log_dt), 'Dskip': _w(Dskip)}
    _A_in = _A_setup()

    def _B_setup():
        B, S, D, F = (1, 2048, 512, 1792)
        Fh = F // 2
        return {'x': _w((lambda *s: torch.randn(*s, dtype=torch.bfloat16) * 0.05)(B, S, D)), 'w_in': _w((lambda *s: torch.randn(*s, dtype=torch.bfloat16) * 0.4)(D, 2 * Fh)), 'w_sp': _w(0.01 * (lambda *s: torch.randn(*s, dtype=torch.bfloat16) * 0.4)(S, S)), 'b_sp': _w(1.0 + 0.01 * (lambda *s: torch.randn(*s, dtype=torch.bfloat16) * 0.05)(S)), 'w_out': _w((lambda *s: torch.randn(*s, dtype=torch.bfloat16) * 0.05)(Fh, D))}
    _B_in = _B_setup()
    return {'A_u': _A_in['u'], 'A_A_re': _A_in['A_re'], 'A_A_im': _A_in['A_im'], 'A_B_re': _A_in['B_re'], 'A_B_im': _A_in['B_im'], 'A_C_re': _A_in['C_re'], 'A_C_im': _A_in['C_im'], 'A_log_dt': _A_in['log_dt'], 'A_Dskip': _A_in['Dskip'], 'B_x': _B_in['x'], 'B_w_in': _B_in['w_in'], 'B_w_sp': _B_in['w_sp'], 'B_b_sp': _B_in['b_sp'], 'B_w_out': _B_in['w_out']}

def reference(A_u, A_A_re, A_A_im, A_B_re, A_B_im, A_C_re, A_C_im, A_log_dt, A_Dskip, B_x, B_w_in, B_w_sp, B_b_sp, B_w_out):

    def _A_ref(u, A_re, A_im, B_re, B_im, C_re, C_im, log_dt, Dskip):
        Bsz, L, D = u.shape
        dt = torch.exp(log_dt)[:, None]
        A = A_re + 1j * A_im
        Bc = B_re + 1j * B_im
        Cc = C_re + 1j * C_im
        logdA = dt * A
        dB = dt * Bc
        l = (lambda x, dt: x.to(dt))(torch.arange(L), A_re.dtype)
        P = torch.exp(logdA[:, :, None] * l[None, None, :])
        K = torch.einsum('dn,dnl,dn->dl', Cc, P, dB).real
        L2 = 2 * L
        Kp = _pad(K, ((0, 0), (0, L)))
        up = (lambda x, *d: x.permute(*d))(u, 0, 2, 1)
        up = _pad(up, ((0, 0), (0, 0), (0, L)))
        Kf = (lambda x, axes=(-2, -1): torch.fft.rfft2(x.float(), dim=axes))(Kp[:, None, :], axes=(-2, -1))
        Uf = (lambda x, axes=(-2, -1): torch.fft.rfft2(x.float(), dim=axes))(up[:, :, None, :], axes=(-2, -1))
        Yf = Uf * Kf[None, :, :, :]
        y = (lambda x, s, axes=(-2, -1): torch.fft.irfft2(x, s=s, dim=axes).to(torch.bfloat16))(Yf, (1, L2), axes=(-2, -1))[:, :, 0, :L]
        y = (lambda x, *d: x.permute(*d))(y, 0, 2, 1) + u * Dskip[None, None, :]
        return y

    def _B_ref(x, w_in, w_sp, b_sp, w_out):
        B, S, D = x.shape
        F = w_in.shape[-1]
        Fh = F // 2
        z = _gelu(torch.einsum('bsd,df->bsf', x, w_in))
        z1 = z[..., :Fh].clone()
        z2 = _layernorm(z[..., Fh:], axis=-1)
        g = torch.einsum('ts,bsf->btf', w_sp, z2) + b_sp[None, :, None]
        return torch.einsum('bsf,fd->bsd', z1 * g, w_out)
    x = A_u
    xn = _layernorm(x, axis=-1)
    return x + _A_ref(xn, A_A_re, A_A_im, A_B_re, A_B_im, A_C_re, A_C_im, A_log_dt, A_Dskip) + _B_ref(xn, B_w_in, B_w_sp, B_b_sp, B_w_out)
```

```cpp
#include <hip/hip_runtime.h>
#include <hip/hip_bf16.h>
#include <stdint.h>

typedef unsigned short u16;
__device__ __forceinline__ float bf2f(u16 v){ return __uint_as_float(((unsigned)v)<<16); }
__device__ __forceinline__ u16 f2bf(float f){ __hip_bfloat16 b = __float2bfloat16(f); return *(u16*)&b; }
__device__ __forceinline__ float rbf(float f){ return bf2f(f2bf(f)); }

constexpr int L=2048, D=512, N=128, F=1792, FH=896;

__global__ void k_ln(const u16* x, u16* xn){
  int t=blockIdx.x; int tid=threadIdx.x;
  __shared__ float red[256];
  float a=bf2f(x[t*D+tid]), b=bf2f(x[t*D+tid+256]);
  red[tid]=a+b; __syncthreads();
  for(int s=128;s>0;s>>=1){ if(tid<s) red[tid]+=red[tid+s]; __syncthreads(); }
  float m=red[0]/D; __syncthreads();
  red[tid]=(a-m)*(a-m)+(b-m)*(b-m); __syncthreads();
  for(int s=128;s>0;s>>=1){ if(tid<s) red[tid]+=red[tid+s]; __syncthreads(); }
  float v=red[0]/D;
  float mb=rbf(m), vb=rbf(v);
  float sd=rbf(sqrtf(rbf(vb+1e-5f)));
  xn[t*D+tid]=f2bf(rbf(a-mb)/sd);
  xn[t*D+tid+256]=f2bf(rbf(b-mb)/sd);
}
__global__ void k_kgen(const u16* Are,const u16* Aim,const u16* Bre,const u16* Bim,const u16* Cre,const u16* Cim,const u16* logdt,float* K){
  int d=blockIdx.x;
  __shared__ float zr[N],zi[N],cr[N],ci[N];
  int tid=threadIdx.x;
  float dt=rbf(expf(bf2f(logdt[d])));
  if(tid<N){
    int i=d*N+tid;
    zr[tid]=dt*bf2f(Are[i]); zi[tid]=dt*bf2f(Aim[i]);
    float br=dt*bf2f(Bre[i]), bi=dt*bf2f(Bim[i]);
    float c_r=bf2f(Cre[i]), c_i=bf2f(Cim[i]);
    cr[tid]=c_r*br-c_i*bi; ci[tid]=c_r*bi+c_i*br;
  }
  __syncthreads();
  for(int l=tid;l<L;l+=blockDim.x){
    float lf=rbf((float)l);
    float acc=0;
    for(int n=0;n<N;n++){
      float e=expf(zr[n]*lf); float s,c; sincosf(zi[n]*lf,&s,&c);
      acc+=e*(cr[n]*c-ci[n]*s);
    }
    K[d*L+l]=acc;
  }
}
__global__ void k_conv(const float* K,const u16* xn,const u16* Dskip,u16* ya){
  int d=blockIdx.x; int tid=threadIdx.x;
  __shared__ float ks[L], us[L];
  for(int i=tid;i<L;i+=blockDim.x){ ks[i]=K[d*L+i]; us[i]=bf2f(xn[i*D+d]); }
  __syncthreads();
  float ds=bf2f(Dskip[d]);
  for(int t=tid;t<L;t+=blockDim.x){
    float acc=0;
    for(int l=0;l<=t;l++) acc+=ks[l]*us[t-l];
    ya[t*D+d]=f2bf(rbf(acc)+rbf(us[t]*ds));
  }
}
template<int MODE>
__global__ void k_gemm(const u16* A,const u16* B,int M,int Nn,int Kk,int lda,int ldb,
                       u16* out,int ldo,const u16* e1,const u16* e2,const u16* e3){
  __shared__ float As[16][17],Bs[16][17];
  int tx=threadIdx.x,ty=threadIdx.y;
  int row=blockIdx.y*16+ty,col=blockIdx.x*16+tx;
  float acc=0;
  for(int k0=0;k0<Kk;k0+=16){
    As[ty][tx]=bf2f(A[(size_t)row*lda+k0+tx]);
    Bs[ty][tx]=bf2f(B[(size_t)(k0+ty)*ldb+col]);
    __syncthreads();
    for(int k=0;k<16;k++) acc+=As[ty][k]*Bs[k][tx];
    __syncthreads();
  }
  if(MODE==0){
    float x=rbf(acc);
    float u=0.7978845608028654f*(x+0.044715f*x*x*x);
    float g=0.5f*x*(1.f+tanhf(u));
    out[(size_t)row*ldo+col]=f2bf(g);
  } else if(MODE==1){
    float g=rbf(rbf(acc)+bf2f(e2[row]));
    out[(size_t)row*ldo+col]=f2bf(bf2f(e1[(size_t)row*F+col])*g);
  } else {
    float b=rbf(acc);
    float s=rbf(bf2f(e1[(size_t)row*D+col])+bf2f(e2[(size_t)row*D+col]));
    out[(size_t)row*ldo+col]=f2bf(s+b);
  }
}
__global__ void k_ln2(const u16* z,u16* z2n){
  int t=blockIdx.x; int tid=threadIdx.x;
  __shared__ float red[128];
  float v[7]; float s=0;
  for(int i=0;i<7;i++){ v[i]=bf2f(z[(size_t)t*F+FH+tid+128*i]); s+=v[i]; }
  red[tid]=s; __syncthreads();
  for(int k=64;k>0;k>>=1){ if(tid<k) red[tid]+=red[tid+k]; __syncthreads(); }
  float m=red[0]/FH; __syncthreads();
  s=0; for(int i=0;i<7;i++) s+=(v[i]-m)*(v[i]-m);
  red[tid]=s; __syncthreads();
  for(int k=64;k>0;k>>=1){ if(tid<k) red[tid]+=red[tid+k]; __syncthreads(); }
  float var=red[0]/FH;
  float mb=rbf(m), sd=rbf(sqrtf(rbf(rbf(var)+1e-5f)));
  for(int i=0;i<7;i++) z2n[(size_t)t*FH+tid+128*i]=f2bf(rbf(v[i]-mb)/sd);
}

extern "C" void kernel_launch(void* const* d_in, const int* in_sizes, int n_in,
                              void* d_out, int out_size, void* d_ws, size_t ws_size,
                              hipStream_t stream) {
  const u16* x=(const u16*)d_in[0];
  const u16 *Are=(const u16*)d_in[1],*Aim=(const u16*)d_in[2],*Bre=(const u16*)d_in[3],*Bim=(const u16*)d_in[4],*Cre=(const u16*)d_in[5],*Cim=(const u16*)d_in[6],*logdt=(const u16*)d_in[7],*Dskip=(const u16*)d_in[8];
  const u16 *w_in=(const u16*)d_in[10],*w_sp=(const u16*)d_in[11],*b_sp=(const u16*)d_in[12],*w_out=(const u16*)d_in[13];
  char* ws=(char*)d_ws;
  u16* xn=(u16*)ws; ws+=L*D*2;
  float* K=(float*)ws; ws+=D*L*4;
  u16* ya=(u16*)ws; ws+=L*D*2;
  u16* z=(u16*)ws; ws+=(size_t)L*F*2;
  u16* z2n=(u16*)ws; ws+=(size_t)L*FH*2;
  u16* h=(u16*)ws; ws+=(size_t)L*FH*2;
  k_ln<<<L,256,0,stream>>>(x,xn);
  k_kgen<<<D,256,0,stream>>>(Are,Aim,Bre,Bim,Cre,Cim,logdt,K);
  k_conv<<<D,256,0,stream>>>(K,xn,Dskip,ya);
  k_gemm<0><<<dim3(F/16,L/16),dim3(16,16),0,stream>>>(xn,w_in,L,F,D,D,F,z,F,nullptr,nullptr,nullptr);
  k_ln2<<<L,128,0,stream>>>(z,z2n);
  k_gemm<1><<<dim3(FH/16,L/16),dim3(16,16),0,stream>>>(w_sp,z2n,L,FH,L,L,FH,h,FH,z,b_sp,nullptr);
  k_gemm<2><<<dim3(D/16,L/16),dim3(16,16),0,stream>>>(h,w_out,L,D,FH,FH,D,(u16*)d_out,D,x,ya,nullptr);
}
```

```cpp
#include <hip/hip_runtime.h>
#include <hip/hip_bf16.h>
#include <stdint.h>

typedef unsigned short u16;
typedef __bf16 bf16x2 __attribute__((ext_vector_type(2)));
typedef __bf16 bf16x8 __attribute__((ext_vector_type(8)));
typedef short s16x4 __attribute__((ext_vector_type(4)));
typedef float f32x2 __attribute__((ext_vector_type(2)));
typedef float f32x4 __attribute__((ext_vector_type(4)));
typedef float f32x16 __attribute__((ext_vector_type(16)));
typedef unsigned u32x2 __attribute__((ext_vector_type(2)));
typedef unsigned u32x4 __attribute__((ext_vector_type(4)));
#define LAS __attribute__((address_space(3)))
#define DEVI __device__ __forceinline__

constexpr int L = 2048, D = 512, F = 1792, FH = 896, NS = 128;
constexpr size_t OFF_Z1F = 0;
constexpr size_t OFF_Z2F = OFF_Z1F + (size_t)128 * 8 * 7 * 512;
constexpr size_t OFF_ST2 = OFF_Z2F + (size_t)8 * 64 * 7 * 1024;
constexpr size_t OFF_WF = OFF_ST2 + (size_t)2048 * 8 * 8;
constexpr size_t OFF_WOF = OFF_WF + (size_t)32 * 64 * 4 * 1024;
constexpr size_t OFF_XNT = OFF_WOF + (size_t)8 * 28 * 4 * 1024;
constexpr size_t OFF_HF = OFF_XNT + (size_t)512 * 2048 * 2;
constexpr size_t OFF_YAT = OFF_HF + (size_t)128 * 28 * 1024;

#define DIAG_BEGIN()
#define DIAG_END(k)
#define STAMP(kk,i)
DEVI unsigned pk2(float a, float b) { f32x2 v = {a, b}; bf16x2 r = __builtin_convertvector(v, bf16x2); return __builtin_bit_cast(unsigned, r); }
DEVI float lo2f(unsigned u) { return __builtin_bit_cast(float, u << 16); }
DEVI float hi2f(unsigned u) { return __builtin_bit_cast(float, u & 0xffff0000u); }
DEVI float rbf(float a) { return hi2f(pk2(a, a)); }
DEVI float bf2f(u16 v) { return __builtin_bit_cast(float, ((unsigned)v) << 16); }
DEVI float dot2(unsigned a, unsigned b, float c) { return __builtin_amdgcn_fdot2_f32_bf16(__builtin_bit_cast(bf16x2, a), __builtin_bit_cast(bf16x2, b), c, false); }
DEVI f32x4 mfma16(u32x4 a, u32x4 b, f32x4 c) { return __builtin_amdgcn_mfma_f32_16x16x32_bf16(__builtin_bit_cast(bf16x8, a), __builtin_bit_cast(bf16x8, b), c, 0, 0, 0); }
DEVI f32x16 mfma32(u32x4 a, u32x4 b, f32x16 c) { return __builtin_amdgcn_mfma_f32_32x32x16_bf16(__builtin_bit_cast(bf16x8, a), __builtin_bit_cast(bf16x8, b), c, 0, 0, 0); }
DEVI float gelu_tanh(float x) {
  float e = __builtin_amdgcn_exp2f(x * (-2.3022082f + -0.10294324f * x * x));
  return x * __builtin_amdgcn_rcpf(1.0f + e);
}
template <int CTRL> DEVI float dpp_mov(float v) { return __builtin_bit_cast(float, __builtin_amdgcn_update_dpp(0, __builtin_bit_cast(int, v), CTRL, 0xf, 0xf, false)); }
DEVI float row16_sum(float v) {
  v += dpp_mov<0x128>(v); v += dpp_mov<0x124>(v); v += dpp_mov<0x122>(v); v += dpp_mov<0x121>(v); return v;
}
DEVI void ln_stats(float s, float ss, float inv_n, float& mb, float& sdb) {
  float m = s * inv_n; float v = fmaxf(ss * inv_n - m * m, 0.0f);
  mb = rbf(m); sdb = rbf(__builtin_amdgcn_sqrtf(rbf(rbf(v) + 1e-5f)));
}
#define WAIT_VM(n) asm volatile("s_waitcnt vmcnt(" #n ")" ::: "memory")
#define WAIT_LGKM0() asm volatile("s_waitcnt lgkmcnt(0)" ::: "memory")
#define BAR() __builtin_amdgcn_s_barrier()
DEVI void glds16(const void* src, char* lds) { __builtin_amdgcn_global_load_lds((const unsigned*)src, (unsigned*)lds, 16, 0, 0); }

constexpr int K1_STAGE = 32768;
constexpr int K1_MISC = 4 * K1_STAGE;
constexpr int K1_LDS = K1_MISC + 2048   + 512   + 2048  ;

template <int HALF>
DEVI void k1_body(const u16* __restrict__ x, const u16* __restrict__ w_in, char* __restrict__ ws, char* smem, int rt, int ct, int w, int lane) {
  const int wm = w & 3, wn = w >> 2;
  const int f0 = HALF * FH + ct * 112;
  const u16* xs0 = x + (size_t)(128 * rt + 16 * w + (lane >> 3)) * D + (((lane & 7) ^ ((lane >> 3) & 3)) * 8);
  const u16* xs1 = x + (size_t)(128 * rt + 16 * w + 8 + (lane >> 3)) * D + (((lane & 7) ^ (((lane >> 3) & 3) | 4)) * 8);
  const int wpair = ((lane & 15) >> 1) ^ (((lane >> 4) & 3) | ((w & 1) << 2));
  const bool wact = wpair < 7;
  const u16* wsrc = w_in + (size_t)(8 * w + (lane >> 4)) * F + f0 + wpair * 16 + (lane & 1) * 8;
  auto stage = [&](int j) {
    char* sb = smem + (j & 3) * K1_STAGE;
    glds16(xs0 + 64 * j, sb + (2 * w) * 1024);
    glds16(xs1 + 64 * j, sb + (2 * w + 1) * 1024);
    if (wact) {
      glds16(wsrc + (size_t)(64 * j) * F, sb + 16384 + (2 * w) * 1024);
      glds16(wsrc + (size_t)(64 * j + 4) * F, sb + 16384 + (2 * w + 1) * 1024);
    }
  };
  STAMP(0,0);
  stage(0); stage(1); stage(2); stage(3);
  STAMP(0,1);
  const int r = lane & 15, q = lane >> 4;
  int xoff[2][2];
#pragma unroll
  for (int i = 0; i < 2; i++) {
    const int m = i ^ wn;
    const int row = 32 * wm + (HALF ? (8 * (r >> 2) + (r & 3) + 4 * m) : (16 * m + r));
    const int s = (row & 3) | (((row >> 3) & 1) << 2);
    xoff[i][0] = row * 128 + ((q ^ s) * 16);
    xoff[i][1] = row * 128 + (((4 + q) ^ s) * 16);
  }
  int woff[4];
  {
    const int qq = (lane & 15) >> 2, p = lane & 3, swz = qq | ((q & 1) << 2);
#pragma unroll
    for (int i = 0; i < 4; i++) {
      const int n = 4 * wn + ((i + wm) & 3);
      woff[i] = 16384 + (8 * q + qq) * 256 + ((n ^ swz) * 32) + 8 * p;
    }
  }
  f32x4 acc[2][4];
#pragma unroll
  for (int i = 0; i < 2; i++)
#pragma unroll
    for (int n = 0; n < 4; n++) acc[i][n] = (f32x4){0.f, 0.f, 0.f, 0.f};
  float ssum = 0.f, ssq = 0.f, cwa = 0.f;
  const unsigned ONES = 0x3f803f80u;
#pragma unroll 1
  for (int j = 0; j < 8; j++) {
    if (j == 0) WAIT_VM(12); else if (j < 6) WAIT_VM(8); else if (j == 6) WAIT_VM(4); else WAIT_VM(0);
    BAR();
    if (j == 0) STAMP(0,2); if (j == 4) STAMP(0,3);
    if (j >= 1 && j <= 4) stage(j + 3);
    const char* sb = smem + (j & 3) * K1_STAGE;
#pragma unroll
    for (int kk = 0; kk < 2; kk++) {
      u32x4 xf[2], wf[4];
#pragma unroll
      for (int i = 0; i < 2; i++) xf[i] = *(const u32x4*)(sb + xoff[i][kk]);
#pragma unroll
      for (int i = 0; i < 4; i++) {
        s16x4 a = __builtin_amdgcn_ds_read_tr16_b64_v4i16((LAS s16x4*)(sb + woff[i] + kk * 8192));
        s16x4 b = __builtin_amdgcn_ds_read_tr16_b64_v4i16((LAS s16x4*)(sb + woff[i] + kk * 8192 + 1024));
        u32x2 a2 = __builtin_bit_cast(u32x2, a), b2 = __builtin_bit_cast(u32x2, b);
        wf[i] = (u32x4){a2[0], a2[1], b2[0], b2[1]};
      }
#pragma unroll
      for (int e = 0; e < 4; e++) { ssum = dot2(xf[0][e], ONES, ssum); ssq = dot2(xf[0][e], xf[0][e], ssq); cwa = dot2(wf[0][e], ONES, cwa); }
#pragma unroll
      for (int i = 0; i < 2; i++)
#pragma unroll
        for (int n = 0; n < 4; n++) acc[i][n] = HALF ? mfma16(xf[i], wf[n], acc[i][n]) : mfma16(wf[n], xf[i], acc[i][n]);
    }
  }
  STAMP(0,4);
  f32x4* rstat = (f32x4*)(smem + K1_MISC);
  float* cwt = (float*)(smem + K1_MISC + 2048);
  f32x2* part = (f32x2*)(smem + K1_MISC + 2560);
  ssum += __shfl_xor(ssum, 16); ssum += __shfl_xor(ssum, 32);
  ssq += __shfl_xor(ssq, 16); ssq += __shfl_xor(ssq, 32);
  cwa += __shfl_xor(cwa, 16); cwa += __shfl_xor(cwa, 32);
  if (q == 0) {
    const int row = 32 * wm + (HALF ? (8 * (r >> 2) + (r & 3) + 4 * wn) : (16 * wn + r));
    float mb, sdb; ln_stats(ssum, ssq, 1.0f / D, mb, sdb);
    const float ri = 1.0f / sdb;
    if ((w & 3) == wm) rstat[row] = (f32x4){ri, ri * mb, mb, sdb};
    cwt[16 * (4 * wn + wm) + r] = cwa;
  }
  __syncthreads();
  if (HALF == 0) {
    char* z1 = ws + OFF_Z1F;
#pragma unroll
    for (int i = 0; i < 2; i++) {
      const int m = i ^ wn;
      const f32x4 rs = rstat[32 * wm + 16 * m + r];
      const int T = 8 * rt + 2 * wm + m;
#pragma unroll
      for (int n = 0; n < 4; n++) {
        const int nt = 4 * wn + ((n + wm) & 3);
        if (nt < 7) {
          const f32x4 cw = *(const f32x4*)(cwt + 16 * nt + 4 * q);
          float v[4];
#pragma unroll
          for (int e = 0; e < 4; e++) v[e] = gelu_tanh(rbf(rs[0] * acc[i][n][e] - rs[1] * cw[e]));
          u32x2 o = {pk2(v[0], v[1]), pk2(v[2], v[3])};
          *(u32x2*)(z1 + ((size_t)((T * 8 + ct) * 7 + nt) * 64 + lane) * 8) = o;
        }
      }
    }
  } else {
    char* z2 = ws + OFF_Z2F;
    float s1[8], s2[8];
#pragma unroll
    for (int e = 0; e < 8; e++) { s1[e] = 0.f; s2[e] = 0.f; }
    f32x2 rs[8];
#pragma unroll
    for (int e = 0; e < 8; e++) { const f32x4 t4 = rstat[32 * wm + 8 * q + e]; rs[e] = (f32x2){t4[0], t4[1]}; }
    const int sig = 4 * rt + wm;
#pragma unroll
    for (int n = 0; n < 4; n++) {
      const int nt = 4 * wn + ((n + wm) & 3);
      if (nt < 7) {
        const float cw = cwt[16 * nt + r];
        unsigned o[4];
#pragma unroll
        for (int m = 0; m < 2; m++) {
          const f32x4 aa = acc[m][n], ab = acc[m ^ 1][n];
          f32x4 a = wn ? ab : aa;
          float v[4];
#pragma unroll
          for (int e = 0; e < 4; e++) v[e] = gelu_tanh(rbf(rs[4 * m + e][0] * a[e] - rs[4 * m + e][1] * cw));
          o[2 * m] = pk2(v[0], v[1]); o[2 * m + 1] = pk2(v[2], v[3]);
          float z0 = lo2f(o[2 * m]), z1v = hi2f(o[2 * m]), z2v = lo2f(o[2 * m + 1]), z3 = hi2f(o[2 * m + 1]);
          s1[4 * m + 0] += z0; s2[4 * m + 0] += z0 * z0;
          s1[4 * m + 1] += z1v; s2[4 * m + 1] += z1v * z1v;
          s1[4 * m + 2] += z2v; s2[4 * m + 2] += z2v * z2v;
          s1[4 * m + 3] += z3; s2[4 * m + 3] += z3 * z3;
        }
        *(u32x4*)(z2 + ((size_t)((ct * 64 + sig) * 7 + nt) * 64 + lane) * 16) = (u32x4){o[0], o[1], o[2], o[3]};
      }
    }
#pragma unroll
    for (int e = 0; e < 8; e++) { s1[e] = row16_sum(s1[e]); s2[e] = row16_sum(s2[e]); }
    if (r == 0) {
#pragma unroll
      for (int e = 0; e < 8; e++) part[wn * 128 + 32 * wm + 8 * q + e] = (f32x2){s1[e], s2[e]};
    }
    __syncthreads();
    const int tid = w * 64 + lane;
    if (tid < 128) {
      const f32x2 a = part[tid], b = part[128 + tid];
      *(f32x2*)(ws + OFF_ST2 + ((size_t)(128 * rt + tid) * 8 + ct) * 8) = (f32x2){a[0] + b[0], a[1] + b[1]};
    }
  }
  STAMP(0,5);
  {
    const int tid = w * 64 + lane, c16 = HALF * 8 + ct;
    const int row = tid >> 2, ch8 = (tid & 3) * 8;
    const u32x4 xv = *(const u32x4*)(x + (size_t)(128 * rt + row) * D + 32 * c16 + ch8);
    const f32x4 rs = rstat[row];
    u16* tl = (u16*)smem;
#pragma unroll
    for (int e = 0; e < 4; e++) {
      const float a = rbf(lo2f(xv[e]) - rs[2]) * rs[0], b = rbf(hi2f(xv[e]) - rs[2]) * rs[0];
      const unsigned p = pk2(a, b);
      tl[(ch8 + 2 * e) * 136 + row] = (u16)p; tl[(ch8 + 2 * e + 1) * 136 + row] = (u16)(p >> 16);
    }
    __syncthreads();
    const int ch = tid >> 4, pc = tid & 15;
    const u32x4 o = *(const u32x4*)(tl + ch * 136 + pc * 8);
    *(u32x4*)(ws + OFF_XNT + ((size_t)(32 * c16 + ch) * L + 128 * rt + pc * 8) * 2) = o;
  }
}

__global__ __launch_bounds__(512, 2) void k1_kernel(const u16* __restrict__ x, const u16* __restrict__ w_in, const u16* __restrict__ w_sp,
                                                    const u16* __restrict__ w_out, char* __restrict__ ws) {
  extern __shared__ __attribute__((aligned(16))) char smem[];
  DIAG_BEGIN();
  const int b = blockIdx.x, xcd = b & 7, i = b >> 3;
  const int tid = threadIdx.x, lane = tid & 63, w = __builtin_amdgcn_readfirstlane(tid >> 6);
  u32x4 wv[4];
  const int wr = 8 * (xcd >> 1) + (i & 7), qd = (xcd & 1) * 4 + (i >> 3);
  {
#pragma unroll
    for (int u = 0; u < 4; u++) {
      const int idx = u * 8 + w;
      const int rg = idx & 7, cg = idx >> 3;
      wv[u] = *(const u32x4*)(w_sp + (size_t)(64 * wr + 8 * rg + (lane >> 3)) * L + 256 * qd + 64 * cg + (lane & 7) * 8);
    }
  }
  unsigned wo[8];
  const bool do_wo = (b < 224) && (tid < 256);
  if (do_wo) {
    const int fid = 4 * b + w, n = fid & 3, s = (fid >> 2) % 28, dt = fid / 112;
    const int r = lane & 15, q = lane >> 4;
    const int d = 64 * dt + 32 * (n >> 1) + 8 * (r >> 2) + (r & 3) + 4 * (n & 1);
#pragma unroll
    for (int e = 0; e < 8; e++) wo[e] = w_out[(size_t)(32 * s + 16 * (e >> 2) + 4 * q + (e & 3)) * D + d];
  }
  const int rt = 4 * (xcd >> 1) + (i & 3), cidx = i >> 2, ct = 4 * (xcd & 1) + (cidx & 3);
  if (cidx >> 2) k1_body<1>(x, w_in, ws, smem, rt, ct, w, lane);
  else k1_body<0>(x, w_in, ws, smem, rt, ct, w, lane);
  STAMP(0,6);
#pragma unroll
  for (int u = 0; u < 4; u++) {
    const int idx = u * 8 + w, rg = idx & 7, cg = idx >> 3;
    const int row = 8 * rg + (lane >> 3), ch = lane & 7;
    const int sig = 8 * qd + 2 * cg + (ch >> 2), q = ch & 3, m = row >> 4, c = row & 15;
    *(u32x4*)(ws + OFF_WF + ((size_t)((wr * 64 + sig) * 4 + m) * 64 + q * 16 + c) * 16) = wv[u];
  }
  if (do_wo) {
    const int fid = 4 * b + w;
#pragma unroll
    for (int e = 0; e < 8; e++) asm volatile("" : "+v"(wo[e]));
    u32x4 o = {wo[0] | (wo[1] << 16), wo[2] | (wo[3] << 16), wo[4] | (wo[5] << 16), wo[6] | (wo[7] << 16)};
    *(u32x4*)(ws + OFF_WOF + ((size_t)fid * 64 + lane) * 16) = o;
  }
  STAMP(0,7);
  DIAG_END(0);
}

constexpr int K2_LDS = 52 * 1024;

DEVI void k2_gemm(const u16* __restrict__ b_sp, char* __restrict__ ws, char* smem, int b, int w, int lane) {
  const int xcd = b & 7, i0 = b >> 3, rt = 8 * (xcd >> 1) + (i0 & 7), ct = 4 * (xcd & 1) + (i0 >> 3);
  const int c = lane & 15, q = lane >> 4;
  const char* wf = ws + OFF_WF + (size_t)rt * 64 * 4096 + lane * 16;
  const char* zf = ws + OFF_Z2F + (size_t)ct * 64 * 7168 + lane * 16;
  const char* st = ws + OFF_ST2 + (size_t)(64 * w + lane) * 64;
  int moff[4];
#pragma unroll
  for (int i = 0; i < 4; i++) moff[i] = ((i + w) & 3) * 1024;
  float* rbuf = (float*)(smem + 49152 + w * 512);
  u16* mbuf = (u16*)(smem + 49152 + w * 512 + 256);
  f32x4 acc[4][7];
#pragma unroll
  for (int i = 0; i < 4; i++)
#pragma unroll
    for (int n = 0; n < 7; n++) acc[i][n] = (f32x4){0.f, 0.f, 0.f, 0.f};
  float cacc[4] = {0.f, 0.f, 0.f, 0.f};
  u32x4 aw[4], az[7], bw[4], bz[7];
  f32x4 sv[4];
  auto loadf = [&](u32x4* fw, u32x4* fz, int sig) {
#pragma unroll
    for (int i = 0; i < 4; i++) fw[i] = *(const u32x4*)(wf + (size_t)sig * 4096 + moff[i]);
#pragma unroll
    for (int n = 0; n < 7; n++) fz[n] = *(const u32x4*)(zf + (size_t)sig * 7168 + n * 1024);
  };
  auto loadst = [&](int j) {
#pragma unroll
    for (int e = 0; e < 4; e++) sv[e] = *(const f32x4*)(st + (size_t)j * 256 * 64 + e * 16);
  };
  auto finstats = [&]() {
    const float s1 = (sv[0][0] + sv[0][2]) + (sv[1][0] + sv[1][2]) + (sv[2][0] + sv[2][2]) + (sv[3][0] + sv[3][2]);
    const float s2 = (sv[0][1] + sv[0][3]) + (sv[1][1] + sv[1][3]) + (sv[2][1] + sv[2][3]) + (sv[3][1] + sv[3][3]);
    float mb, sdb; ln_stats(s1, s2, 1.0f / FH, mb, sdb);
    rbuf[lane] = __builtin_amdgcn_rcpf(sdb);
    mbuf[lane] = (u16)(__builtin_bit_cast(unsigned, mb) >> 16);
  };
  auto compute = [&](const u32x4* fw, const u32x4* fz, int e) {
    const f32x4 r0 = *(const f32x4*)(rbuf + 32 * e + 8 * q), r1 = *(const f32x4*)(rbuf + 32 * e + 8 * q + 4);
    const u32x4 mp = *(const u32x4*)(mbuf + 32 * e + 8 * q);
#pragma unroll
    for (int i = 0; i < 4; i++) {
      u32x4 wsx;
      wsx[0] = pk2(lo2f(fw[i][0]) * r0[0], hi2f(fw[i][0]) * r0[1]);
      wsx[1] = pk2(lo2f(fw[i][1]) * r0[2], hi2f(fw[i][1]) * r0[3]);
      wsx[2] = pk2(lo2f(fw[i][2]) * r1[0], hi2f(fw[i][2]) * r1[1]);
      wsx[3] = pk2(lo2f(fw[i][3]) * r1[2], hi2f(fw[i][3]) * r1[3]);
#pragma unroll
      for (int e2 = 0; e2 < 4; e2++) cacc[i] = dot2(wsx[e2], mp[e2], cacc[i]);
#pragma unroll
      for (int n = 0; n < 7; n++) acc[i][n] = mfma16(fz[n], wsx, acc[i][n]);
    }
  };
  loadf(aw, az, 2 * w);
  loadst(0);
#pragma unroll 1
  for (int j = 0; j < 7; j++) {
    const int sig = 8 * j + 2 * w;
    loadf(bw, bz, sig + 1);
    finstats();
    loadst(j + 1);
    compute(aw, az, 0);
    loadf(aw, az, sig + 8);
    compute(bw, bz, 1);
  }
  loadf(bw, bz, 56 + 2 * w + 1);
  const int T = 4 * rt + w;
  u32x2 z1v[7];
#pragma unroll
  for (int n = 0; n < 7; n++) z1v[n] = *(const u32x2*)(ws + OFF_Z1F + ((size_t)((T * 8 + ct) * 7 + n) * 64 + lane) * 8);
  const float bsp = bf2f(b_sp[16 * T + c]);
  finstats();
  compute(aw, az, 0);
  compute(bw, bz, 1);
  float* cbuf = (float*)(smem + 49152 + 2048);
#pragma unroll
  for (int i = 0; i < 4; i++) { cacc[i] += __shfl_xor(cacc[i], 16); cacc[i] += __shfl_xor(cacc[i], 32); }
  if (q == 0) {
#pragma unroll
    for (int i = 0; i < 4; i++) cbuf[(((w + i) & 3) * 4 + i) * 16 + c] = cacc[i];
  }
#pragma unroll
  for (int i = 1; i < 4; i++)
#pragma unroll
    for (int n = 0; n < 4; n++) *(f32x4*)(smem + ((((w + i) & 3) * 3 + (i - 1)) * 4 + n) * 1024 + lane * 16) = acc[i][n];
  __syncthreads();
#pragma unroll
  for (int i = 0; i < 3; i++)
#pragma unroll
    for (int n = 0; n < 4; n++) acc[0][n] += *(const f32x4*)(smem + ((w * 3 + i) * 4 + n) * 1024 + lane * 16);
  const float cs = cbuf[(w * 4 + 0) * 16 + c] + cbuf[(w * 4 + 1) * 16 + c] + cbuf[(w * 4 + 2) * 16 + c] + cbuf[(w * 4 + 3) * 16 + c];
  __syncthreads();
#pragma unroll
  for (int i = 1; i < 4; i++)
#pragma unroll
    for (int n = 4; n < 7; n++) *(f32x4*)(smem + ((((w + i) & 3) * 3 + (i - 1)) * 4 + (n - 4)) * 1024 + lane * 16) = acc[i][n];
  __syncthreads();
#pragma unroll
  for (int i = 0; i < 3; i++)
#pragma unroll
    for (int n = 4; n < 7; n++) acc[0][n] += *(const f32x4*)(smem + ((w * 3 + i) * 4 + (n - 4)) * 1024 + lane * 16);
#pragma unroll
  for (int n = 0; n < 7; n++) {
    float g[4];
#pragma unroll
    for (int e = 0; e < 4; e++) g[e] = rbf(rbf(acc[0][n][e] - cs) + bsp);
    u32x2 o = {pk2(lo2f(z1v[n][0]) * g[0], hi2f(z1v[n][0]) * g[1]), pk2(lo2f(z1v[n][1]) * g[2], hi2f(z1v[n][1]) * g[3])};
    const int Ft = 7 * ct + n;
    *(u32x2*)(ws + OFF_HF + ((size_t)(T * 28 + (Ft >> 1)) * 64 + lane) * 16 + (Ft & 1) * 8) = o;
  }
}

constexpr int SS_P = 0, SS_U = 4096, SS_UP = 4672, SS_KD = SS_U + 2 * SS_UP, SS_KR = SS_KD + 16384, SS_KP = 4288;
static_assert(SS_KR + 4 * SS_KP <= K2_LDS, "lds");

DEVI void k2_ssm(const u16* __restrict__ Are, const u16* __restrict__ Aim, const u16* __restrict__ Bre, const u16* __restrict__ Bim,
                 const u16* __restrict__ Cre, const u16* __restrict__ Cim, const u16* __restrict__ logdt, const u16* __restrict__ Dskip,
                 char* __restrict__ ws, char* smem, int b2, int w, int lane) {
  const int ch = w >> 1, hf = w & 1, d = 2 * b2 + ch, t128 = hf * 64 + lane;
  {
    const int i = d * NS + t128;
    const float dt = rbf(expf(bf2f(logdt[d])));
    const float zr = dt * bf2f(Are[i]), zi = dt * bf2f(Aim[i]);
    const float br = dt * bf2f(Bre[i]), bi = dt * bf2f(Bim[i]);
    const float cr = bf2f(Cre[i]), ci = bf2f(Cim[i]);
    *(f32x4*)(smem + SS_P + (ch * 128 + t128) * 16) = (f32x4){zr * 1.4426950408889634f, zi * 0.15915494309189535f, cr * br - ci * bi, cr * bi + ci * br};
    char* ub = smem + SS_U + ch * SS_UP;
    const u32x4* src = (const u32x4*)(ws + OFF_XNT + (size_t)d * L * 2);
    *(u32x4*)(ub + 544 + t128 * 16) = src[t128];
    *(u32x4*)(ub + 544 + 2048 + t128 * 16) = src[128 + t128];
    if (t128 < 34) *(u32x4*)(ub + t128 * 16) = (u32x4){0u, 0u, 0u, 0u};
  }
  __syncthreads();
  {
    const int r = lane & 31, h = lane >> 5;
    const float la = (float)(r < 8 ? r : r < 12 ? 256 + 2 * (r - 8) : r < 14 ? 512 + 4 * (r - 12) : r < 16 ? 768 + 4 * (r - 14) : r < 20 ? 1024 + 256 * (r - 16) : r == 20 ? 2048 : 0);
    const float lb = (float)(8 * r);
    f32x16 kacc;
#pragma unroll
    for (int e = 0; e < 16; e++) kacc[e] = 0.f;
#pragma unroll 2
    for (int s = 0; s < 8; s++) {
      u32x4 uf, vf;
#pragma unroll
      for (int e = 0; e < 4; e++) {
        const f32x4 p = *(const f32x4*)(smem + SS_P + (ch * 128 + 64 * hf + 8 * s + 4 * h + e) * 16);
        const float ma = __builtin_amdgcn_exp2f(p[0] * la), pa = __builtin_amdgcn_fractf(p[1] * la);
        const float ca = __builtin_amdgcn_cosf(pa), sa = __builtin_amdgcn_sinf(pa);
        uf[e] = pk2(ma * (p[2] * ca - p[3] * sa), -ma * (p[2] * sa + p[3] * ca));
        const float mb = __builtin_amdgcn_exp2f(p[0] * lb), pb = __builtin_amdgcn_fractf(p[1] * lb);
        vf[e] = pk2(mb * __builtin_amdgcn_cosf(pb), mb * __builtin_amdgcn_sinf(pb));
      }
      kacc = mfma32(uf, vf, kacc);
    }
    float* kd = (float*)(smem + SS_KD + w * 4096);
#pragma unroll
    for (int e = 0; e < 16; e++) kd[((e & 3) + 8 * (e >> 2) + 4 * h) * 32 + r] = kacc[e];
  }
  __syncthreads();
  {
    const float* k0 = (const float*)(smem + SS_KD + (2 * ch) * 4096);
    const float* k1 = k0 + 1024;
    auto kval = [&](int l) -> float {
      if (l < 0 || l >= L) return 0.f;
      const int lf = (int)rbf((float)l);
      const int row = lf < 256 ? (lf & 7) : lf < 512 ? 8 + ((lf & 7) >> 1) : lf < 1024 ? 12 + 2 * ((lf >> 8) & 1) + ((lf >> 2) & 1) : lf < 2048 ? 16 + ((lf >> 8) & 3) : 20;
      const int idx = row * 32 + ((lf & 255) >> 3);
      return k0[idx] + k1[idx];
    };
    unsigned* c0 = (unsigned*)(smem + SS_KR + (2 * ch) * SS_KP);
    unsigned* c1 = (unsigned*)(smem + SS_KR + (2 * ch + 1) * SS_KP);
#pragma unroll 1
    for (int j = t128; j < 1052; j += 128) {
      const int l1 = 2080 - 2 * j;
      const float a = kval(l1), bb = kval(l1 - 1), cc = kval(l1 - 2);
      c0[j] = pk2(a, bb); c1[j] = pk2(bb, cc);
    }
  }
  __syncthreads();
  const int a = lane & 15, q = lane >> 4;
  f32x4 acc[8];
#pragma unroll
  for (int e = 0; e < 8; e++) acc[e] = (f32x4){0.f, 0.f, 0.f, 0.f};
  {
    const char* kb = smem + SS_KR + (2 * ch + (a & 1)) * SS_KP + (2088 - a - 8 * q - (a & 1)) * 2;
    const char* ub = smem + SS_U + ch * SS_UP + 560 + 32 * a - 16 * q;
    auto afrag = [&](int s) -> u32x4 { const unsigned* p = (const unsigned*)(kb - 64 * s); return (u32x4){p[0], p[1], p[2], p[3]}; };
    auto bfrag = [&](int sg) -> u32x4 { return *(const u32x4*)(ub - 64 * sg); };
    if (hf == 0) {
      const u32x4 b8 = bfrag(8);
#pragma unroll
      for (int cc = 0; cc < 8; cc++) acc[cc] = mfma16(afrag(8 * (cc + 1)), b8, acc[cc]);
    }
#pragma unroll 1
    for (int ii = 0; ii < 4; ii++) {
      const int i = 4 * hf + ii;
      u32x4 af[8], bfr[8];
#pragma unroll
      for (int p = 0; p < 8; p++) { af[p] = afrag(8 * p + i); bfr[p] = bfrag(i - 8 * p); }
#pragma unroll
      for (int cc = 0; cc < 8; cc++)
#pragma unroll
        for (int p = 0; p <= cc; p++) acc[cc] = mfma16(af[p], bfr[cc - p], acc[cc]);
    }
  }
  float* red = (float*)(smem + SS_KD);
  if (hf == 0) {
#pragma unroll
    for (int e = 0; e < 4; e++) *(f32x4*)((char*)red + ((ch * 2 + 0) * 4 + e) * 1024 + lane * 16) = acc[4 + e];
  } else {
#pragma unroll
    for (int e = 0; e < 4; e++) *(f32x4*)((char*)red + ((ch * 2 + 1) * 4 + e) * 1024 + lane * 16) = acc[e];
  }
  __syncthreads();
  const float dsk = bf2f(Dskip[d]);
#pragma unroll
  for (int e = 0; e < 4; e++) {
    const f32x4 o4 = *(const f32x4*)((char*)red + ((ch * 2 + (hf ^ 1)) * 4 + e) * 1024 + lane * 16);
    const f32x4 alo = acc[e], ahi = acc[4 + e];
    const f32x4 mine = hf ? ahi : alo;
    const int t = 256 * (4 * hf + e) + 16 * a + 4 * q;
    const u32x2 uv = *(const u32x2*)(smem + SS_U + ch * SS_UP + 544 + t * 2);
    const float y0 = rbf(mine[0] + o4[0]) + rbf(lo2f(uv[0]) * dsk), y1 = rbf(mine[1] + o4[1]) + rbf(hi2f(uv[0]) * dsk);
    const float y2 = rbf(mine[2] + o4[2]) + rbf(lo2f(uv[1]) * dsk), y3 = rbf(mine[3] + o4[3]) + rbf(hi2f(uv[1]) * dsk);
    *(u32x2*)(ws + OFF_YAT + ((size_t)d * L + t) * 2) = (u32x2){pk2(y0, y1), pk2(y2, y3)};
  }
}

__global__ __launch_bounds__(256, 1) void k2_kernel(const u16* __restrict__ b_sp, const u16* __restrict__ Are, const u16* __restrict__ Aim,
                                                    const u16* __restrict__ Bre, const u16* __restrict__ Bim, const u16* __restrict__ Cre,
                                                    const u16* __restrict__ Cim, const u16* __restrict__ logdt, const u16* __restrict__ Dskip,
                                                    char* __restrict__ ws) {
  extern __shared__ __attribute__((aligned(16))) char smem[];
  DIAG_BEGIN();
  const int lane = threadIdx.x & 63, w = __builtin_amdgcn_readfirstlane(threadIdx.x >> 6);
  if (blockIdx.x < 256) k2_gemm(b_sp, ws, smem, blockIdx.x, w, lane);
  else k2_ssm(Are, Aim, Bre, Bim, Cre, Cim, logdt, Dskip, ws, smem, blockIdx.x - 256, w, lane);
  DIAG_END(1);
}

constexpr int K3_LDS = 48 * 1024;
__global__ __launch_bounds__(256, 1) void k3_kernel(const u16* __restrict__ x, char* __restrict__ ws, u16* __restrict__ out) {
  extern __shared__ __attribute__((aligned(16))) char smem[];
  DIAG_BEGIN();
  const int b = blockIdx.x, xcd = b & 7, i0 = b >> 3, rt = 8 * (xcd >> 1) + (i0 & 7), dt = 4 * (xcd & 1) + (i0 >> 3);
  const int lane = threadIdx.x & 63, w = __builtin_amdgcn_readfirstlane(threadIdx.x >> 6);
  const int c = lane & 15, q = lane >> 4;
  u32x4 hf[7][4], wo[7][4];
#pragma unroll
  for (int s = 0; s < 7; s++) {
#pragma unroll
    for (int i = 0; i < 4; i++) hf[s][i] = *(const u32x4*)(ws + OFF_HF + ((size_t)((4 * rt + ((i + w) & 3)) * 28 + 7 * w + s) * 64 + lane) * 16);
#pragma unroll
    for (int n = 0; n < 4; n++) wo[s][n] = *(const u32x4*)(ws + OFF_WOF + ((size_t)((dt * 28 + 7 * w + s) * 4 + n) * 64 + lane) * 16);
  }
  const int t = 64 * rt + 16 * w + c;
  u32x4 xv[2]; unsigned ya[2][8];
#pragma unroll
  for (int P = 0; P < 2; P++) {
    const int d = 64 * dt + 32 * P + 8 * q;
    xv[P] = *(const u32x4*)(x + (size_t)t * D + d);
#pragma unroll
    for (int e = 0; e < 8; e++) ya[P][e] = *(const u16*)(ws + OFF_YAT + ((size_t)(d + e) * L + t) * 2);
  }
  __builtin_amdgcn_sched_barrier(0);
  f32x4 acc[4][4];
#pragma unroll
  for (int i = 0; i < 4; i++)
#pragma unroll
    for (int n = 0; n < 4; n++) acc[i][n] = (f32x4){0.f, 0.f, 0.f, 0.f};
#pragma unroll
  for (int s = 0; s < 7; s++)
#pragma unroll
    for (int i = 0; i < 4; i++)
#pragma unroll
      for (int n = 0; n < 4; n++) acc[i][n] = mfma16(wo[s][n], hf[s][i], acc[i][n]);
#pragma unroll
  for (int i = 1; i < 4; i++)
#pragma unroll
    for (int n = 0; n < 4; n++) *(f32x4*)(smem + ((((w + i) & 3) * 3 + (i - 1)) * 4 + n) * 1024 + lane * 16) = acc[i][n];
  __syncthreads();
#pragma unroll
  for (int i = 0; i < 3; i++)
#pragma unroll
    for (int n = 0; n < 4; n++) acc[0][n] += *(const f32x4*)(smem + ((w * 3 + i) * 4 + n) * 1024 + lane * 16);
#pragma unroll
  for (int P = 0; P < 2; P++) {
    float o[8];
#pragma unroll
    for (int e = 0; e < 8; e++) {
      const float xe = (e & 1) ? hi2f(xv[P][e >> 1]) : lo2f(xv[P][e >> 1]);
      const float bo = rbf(acc[0][2 * P + (e >> 2)][e & 3]);
      o[e] = rbf(xe + __builtin_bit_cast(float, ya[P][e] << 16)) + bo;
    }
    *(u32x4*)(out + (size_t)t * D + 64 * dt + 32 * P + 8 * q) = (u32x4){pk2(o[0], o[1]), pk2(o[2], o[3]), pk2(o[4], o[5]), pk2(o[6], o[7])};
  }
  DIAG_END(2);
}

extern "C" void kernel_launch(void* const* d_in, const int* in_sizes, int n_in, void* d_out, int out_size, void* d_ws, size_t ws_size,
                              hipStream_t stream) {
  const u16* x = (const u16*)d_in[0];
  const u16 *Are = (const u16*)d_in[1], *Aim = (const u16*)d_in[2], *Bre = (const u16*)d_in[3], *Bim = (const u16*)d_in[4];
  const u16 *Cre = (const u16*)d_in[5], *Cim = (const u16*)d_in[6], *logdt = (const u16*)d_in[7], *Dskip = (const u16*)d_in[8];
  const u16* w_in = (const u16*)d_in[10];
  const u16* w_sp = (const u16*)d_in[11];
  const u16* b_sp = (const u16*)d_in[12];
  const u16* w_out = (const u16*)d_in[13];
  char* ws = (char*)d_ws;
  (void)hipFuncSetAttribute((const void*)k1_kernel, hipFuncAttributeMaxDynamicSharedMemorySize, K1_LDS);
  k1_kernel<<<256, 512, K1_LDS, stream>>>(x, w_in, w_sp, w_out, ws);
  k2_kernel<<<512, 256, K2_LDS, stream>>>(b_sp, Are, Aim, Bre, Bim, Cre, Cim, logdt, Dskip, ws);
  k3_kernel<<<256, 256, K3_LDS, stream>>>(x, ws, (u16*)d_out);
}
```

```cpp
#include <hip/hip_runtime.h>
#include <hip/hip_bf16.h>
#include <stdint.h>

typedef unsigned short u16;
typedef __bf16 bf16x2 __attribute__((ext_vector_type(2)));
typedef __bf16 bf16x8 __attribute__((ext_vector_type(8)));
typedef short s16x4 __attribute__((ext_vector_type(4)));
typedef float f32x2 __attribute__((ext_vector_type(2)));
typedef float f32x4 __attribute__((ext_vector_type(4)));
typedef float f32x16 __attribute__((ext_vector_type(16)));
typedef unsigned u32x2 __attribute__((ext_vector_type(2)));
typedef unsigned u32x4 __attribute__((ext_vector_type(4)));
#define LAS __attribute__((address_space(3)))
#define DEVI __device__ __forceinline__

constexpr int L = 2048, D = 512, F = 1792, FH = 896, NS = 128;
constexpr size_t OFF_Z1F = 0;
constexpr size_t OFF_Z2F = OFF_Z1F + (size_t)128 * 8 * 7 * 512;
constexpr size_t OFF_ST2 = OFF_Z2F + (size_t)8 * 64 * 7 * 1024;
constexpr size_t OFF_WF = OFF_ST2 + (size_t)2048 * 8 * 8;
constexpr size_t OFF_WOF = OFF_WF + (size_t)32 * 64 * 4 * 1024;
constexpr size_t OFF_XNT = OFF_WOF + (size_t)8 * 28 * 4 * 1024;
constexpr size_t OFF_HF = OFF_XNT + (size_t)512 * 2048 * 2;
constexpr size_t OFF_YAT = OFF_HF + (size_t)128 * 28 * 1024;

#define DIAG_BEGIN()
#define DIAG_END(k)
#define STAMP(kk,i)
DEVI unsigned pk2(float a, float b) { f32x2 v = {a, b}; bf16x2 r = __builtin_convertvector(v, bf16x2); return __builtin_bit_cast(unsigned, r); }
DEVI float lo2f(unsigned u) { return __builtin_bit_cast(float, u << 16); }
DEVI float hi2f(unsigned u) { return __builtin_bit_cast(float, u & 0xffff0000u); }
DEVI float rbf(float a) { return hi2f(pk2(a, a)); }
DEVI float bf2f(u16 v) { return __builtin_bit_cast(float, ((unsigned)v) << 16); }
DEVI float dot2(unsigned a, unsigned b, float c) { return __builtin_amdgcn_fdot2_f32_bf16(__builtin_bit_cast(bf16x2, a), __builtin_bit_cast(bf16x2, b), c, false); }
DEVI f32x4 mfma16(u32x4 a, u32x4 b, f32x4 c) { return __builtin_amdgcn_mfma_f32_16x16x32_bf16(__builtin_bit_cast(bf16x8, a), __builtin_bit_cast(bf16x8, b), c, 0, 0, 0); }
DEVI f32x16 mfma32(u32x4 a, u32x4 b, f32x16 c) { return __builtin_amdgcn_mfma_f32_32x32x16_bf16(__builtin_bit_cast(bf16x8, a), __builtin_bit_cast(bf16x8, b), c, 0, 0, 0); }
DEVI float gelu_tanh(float x) {
  float e = __builtin_amdgcn_exp2f(x * (-2.3022082f + -0.10294324f * x * x));
  return x * __builtin_amdgcn_rcpf(1.0f + e);
}
template <int CTRL> DEVI float dpp_mov(float v) { return __builtin_bit_cast(float, __builtin_amdgcn_update_dpp(0, __builtin_bit_cast(int, v), CTRL, 0xf, 0xf, false)); }
DEVI float row16_sum(float v) {
  v += dpp_mov<0x128>(v); v += dpp_mov<0x124>(v); v += dpp_mov<0x122>(v); v += dpp_mov<0x121>(v); return v;
}
DEVI void ln_stats(float s, float ss, float inv_n, float& mb, float& sdb) {
  float m = s * inv_n; float v = fmaxf(ss * inv_n - m * m, 0.0f);
  mb = rbf(m); sdb = rbf(__builtin_amdgcn_sqrtf(rbf(rbf(v) + 1e-5f)));
}
#define WAIT_VM(n) asm volatile("s_waitcnt vmcnt(" #n ")" ::: "memory")
#define WAIT_LGKM0() asm volatile("s_waitcnt lgkmcnt(0)" ::: "memory")
#define BAR() __builtin_amdgcn_s_barrier()
DEVI void glds16(const void* src, char* lds) { __builtin_amdgcn_global_load_lds((const unsigned*)src, (unsigned*)lds, 16, 0, 0); }

constexpr int K1_STAGE = 32768;
constexpr int K1_MISC = 4 * K1_STAGE;
constexpr int K1_LDS = K1_MISC + 2048   + 512   + 2048  ;

template <int HALF>
DEVI void k1_body(const u16* __restrict__ x, const u16* __restrict__ w_in, char* __restrict__ ws, char* smem, int rt, int ct, int w, int lane) {
  const int wm = w & 3, wn = w >> 2;
  const int f0 = HALF * FH + ct * 112;
  const u16* xs0 = x + (size_t)(128 * rt + 16 * w + (lane >> 3)) * D + (((lane & 7) ^ ((lane >> 3) & 3)) * 8);
  const u16* xs1 = x + (size_t)(128 * rt + 16 * w + 8 + (lane >> 3)) * D + (((lane & 7) ^ (((lane >> 3) & 3) | 4)) * 8);
  const int wpair = ((lane & 15) >> 1) ^ (((lane >> 4) & 3) | ((w & 1) << 2));
  const bool wact = wpair < 7;
  const u16* wsrc = w_in + (size_t)(8 * w + (lane >> 4)) * F + f0 + wpair * 16 + (lane & 1) * 8;
  auto stage = [&](int j) {
    char* sb = smem + (j & 3) * K1_STAGE;
    glds16(xs0 + 64 * j, sb + (2 * w) * 1024);
    glds16(xs1 + 64 * j, sb + (2 * w + 1) * 1024);
    if (wact) {
      glds16(wsrc + (size_t)(64 * j) * F, sb + 16384 + (2 * w) * 1024);
      glds16(wsrc + (size_t)(64 * j + 4) * F, sb + 16384 + (2 * w + 1) * 1024);
    }
  };
  STAMP(0,0);
  stage(0); stage(1); stage(2); stage(3);
  STAMP(0,1);
  const int r = lane & 15, q = lane >> 4;
  int xoff[2][2];
#pragma unroll
  for (int i = 0; i < 2; i++) {
    const int m = i ^ wn;
    const int row = 32 * wm + (HALF ? (8 * (r >> 2) + (r & 3) + 4 * m) : (16 * m + r));
    const int s = (row & 3) | (((row >> 3) & 1) << 2);
    xoff[i][0] = row * 128 + ((q ^ s) * 16);
    xoff[i][1] = row * 128 + (((4 + q) ^ s) * 16);
  }
  int woff[4];
  {
    const int qq = (lane & 15) >> 2, p = lane & 3, swz = qq | ((q & 1) << 2);
#pragma unroll
    for (int i = 0; i < 4; i++) {
      const int n = 4 * wn + ((i + wm) & 3);
      woff[i] = 16384 + (8 * q + qq) * 256 + ((n ^ swz) * 32) + 8 * p;
    }
  }
  f32x4 acc[2][4];
#pragma unroll
  for (int i = 0; i < 2; i++)
#pragma unroll
    for (int n = 0; n < 4; n++) acc[i][n] = (f32x4){0.f, 0.f, 0.f, 0.f};
  float ssum = 0.f, ssq = 0.f, cwa = 0.f;
  const unsigned ONES = 0x3f803f80u;
#pragma unroll 1
  for (int j = 0; j < 8; j++) {
    if (j == 0) WAIT_VM(12); else if (j < 6) WAIT_VM(8); else if (j == 6) WAIT_VM(4); else WAIT_VM(0);
    BAR();
    if (j == 0) STAMP(0,2); if (j == 4) STAMP(0,3);
    if (j >= 1 && j <= 4) stage(j + 3);
    const char* sb = smem + (j & 3) * K1_STAGE;
#pragma unroll
    for (int kk = 0; kk < 2; kk++) {
      u32x4 xf[2], wf[4];
#pragma unroll
      for (int i = 0; i < 2; i++) xf[i] = *(const u32x4*)(sb + xoff[i][kk]);
#pragma unroll
      for (int i = 0; i < 4; i++) {
        s16x4 a = __builtin_amdgcn_ds_read_tr16_b64_v4i16((LAS s16x4*)(sb + woff[i] + kk * 8192));
        s16x4 b = __builtin_amdgcn_ds_read_tr16_b64_v4i16((LAS s16x4*)(sb + woff[i] + kk * 8192 + 1024));
        u32x2 a2 = __builtin_bit_cast(u32x2, a), b2 = __builtin_bit_cast(u32x2, b);
        wf[i] = (u32x4){a2[0], a2[1], b2[0], b2[1]};
      }
#pragma unroll
      for (int e = 0; e < 4; e++) { ssum = dot2(xf[0][e], ONES, ssum); ssq = dot2(xf[0][e], xf[0][e], ssq); cwa = dot2(wf[0][e], ONES, cwa); }
#pragma unroll
      for (int i = 0; i < 2; i++)
#pragma unroll
        for (int n = 0; n < 4; n++) acc[i][n] = HALF ? mfma16(xf[i], wf[n], acc[i][n]) : mfma16(wf[n], xf[i], acc[i][n]);
    }
  }
  STAMP(0,4);
  f32x4* rstat = (f32x4*)(smem + K1_MISC);
  float* cwt = (float*)(smem + K1_MISC + 2048);
  f32x2* part = (f32x2*)(smem + K1_MISC + 2560);
  ssum += __shfl_xor(ssum, 16); ssum += __shfl_xor(ssum, 32);
  ssq += __shfl_xor(ssq, 16); ssq += __shfl_xor(ssq, 32);
  cwa += __shfl_xor(cwa, 16); cwa += __shfl_xor(cwa, 32);
  if (q == 0) {
    const int row = 32 * wm + (HALF ? (8 * (r >> 2) + (r & 3) + 4 * wn) : (16 * wn + r));
    float mb, sdb; ln_stats(ssum, ssq, 1.0f / D, mb, sdb);
    const float ri = 1.0f / sdb;
    if ((w & 3) == wm) rstat[row] = (f32x4){ri, ri * mb, mb, sdb};
    cwt[16 * (4 * wn + wm) + r] = cwa;
  }
  __syncthreads();
  if (HALF == 0) {
    char* z1 = ws + OFF_Z1F;
#pragma unroll
    for (int i = 0; i < 2; i++) {
      const int m = i ^ wn;
      const f32x4 rs = rstat[32 * wm + 16 * m + r];
      const int T = 8 * rt + 2 * wm + m;
#pragma unroll
      for (int n = 0; n < 4; n++) {
        const int nt = 4 * wn + ((n + wm) & 3);
        if (nt < 7) {
          const f32x4 cw = *(const f32x4*)(cwt + 16 * nt + 4 * q);
          float v[4];
#pragma unroll
          for (int e = 0; e < 4; e++) v[e] = gelu_tanh(rbf(rs[0] * acc[i][n][e] - rs[1] * cw[e]));
          u32x2 o = {pk2(v[0], v[1]), pk2(v[2], v[3])};
          *(u32x2*)(z1 + ((size_t)((T * 8 + ct) * 7 + nt) * 64 + lane) * 8) = o;
        }
      }
    }
  } else {
    char* z2 = ws + OFF_Z2F;
    float s1[8], s2[8];
#pragma unroll
    for (int e = 0; e < 8; e++) { s1[e] = 0.f; s2[e] = 0.f; }
    f32x2 rs[8];
#pragma unroll
    for (int e = 0; e < 8; e++) { const f32x4 t4 = rstat[32 * wm + 8 * q + e]; rs[e] = (f32x2){t4[0], t4[1]}; }
    const int sig = 4 * rt + wm;
#pragma unroll
    for (int n = 0; n < 4; n++) {
      const int nt = 4 * wn + ((n + wm) & 3);
      if (nt < 7) {
        const float cw = cwt[16 * nt + r];
        unsigned o[4];
#pragma unroll
        for (int m = 0; m < 2; m++) {
          const f32x4 aa = acc[m][n], ab = acc[m ^ 1][n];
          f32x4 a = wn ? ab : aa;
          float v[4];
#pragma unroll
          for (int e = 0; e < 4; e++) v[e] = gelu_tanh(rbf(rs[4 * m + e][0] * a[e] - rs[4 * m + e][1] * cw));
          o[2 * m] = pk2(v[0], v[1]); o[2 * m + 1] = pk2(v[2], v[3]);
          float z0 = lo2f(o[2 * m]), z1v = hi2f(o[2 * m]), z2v = lo2f(o[2 * m + 1]), z3 = hi2f(o[2 * m + 1]);
          s1[4 * m + 0] += z0; s2[4 * m + 0] += z0 * z0;
          s1[4 * m + 1] += z1v; s2[4 * m + 1] += z1v * z1v;
          s1[4 * m + 2] += z2v; s2[4 * m + 2] += z2v * z2v;
          s1[4 * m + 3] += z3; s2[4 * m + 3] += z3 * z3;
        }
        *(u32x4*)(z2 + ((size_t)((ct * 64 + sig) * 7 + nt) * 64 + lane) * 16) = (u32x4){o[0], o[1], o[2], o[3]};
      }
    }
#pragma unroll
    for (int e = 0; e < 8; e++) { s1[e] = row16_sum(s1[e]); s2[e] = row16_sum(s2[e]); }
    if (r == 0) {
#pragma unroll
      for (int e = 0; e < 8; e++) part[wn * 128 + 32 * wm + 8 * q + e] = (f32x2){s1[e], s2[e]};
    }
    __syncthreads();
    const int tid = w * 64 + lane;
    if (tid < 128) {
      const f32x2 a = part[tid], b = part[128 + tid];
      *(f32x2*)(ws + OFF_ST2 + ((size_t)(128 * rt + tid) * 8 + ct) * 8) = (f32x2){a[0] + b[0], a[1] + b[1]};
    }
  }
  STAMP(0,5);
  {
    const int tid = w * 64 + lane, c16 = HALF * 8 + ct;
    const int row = tid >> 2, ch8 = (tid & 3) * 8;
    const u32x4 xv = *(const u32x4*)(x + (size_t)(128 * rt + row) * D + 32 * c16 + ch8);
    const f32x4 rs = rstat[row];
    u16* tl = (u16*)smem;
#pragma unroll
    for (int e = 0; e < 4; e++) {
      const float a = rbf(lo2f(xv[e]) - rs[2]) * rs[0], b = rbf(hi2f(xv[e]) - rs[2]) * rs[0];
      const unsigned p = pk2(a, b);
      tl[(ch8 + 2 * e) * 136 + row] = (u16)p; tl[(ch8 + 2 * e + 1) * 136 + row] = (u16)(p >> 16);
    }
    __syncthreads();
    const int ch = tid >> 4, pc = tid & 15;
    const u32x4 o = *(const u32x4*)(tl + ch * 136 + pc * 8);
    *(u32x4*)(ws + OFF_XNT + ((size_t)(32 * c16 + ch) * L + 128 * rt + pc * 8) * 2) = o;
  }
}

extern "C" __global__ __launch_bounds__(512, 2) void k1_kernel(const u16* __restrict__ x, const u16* __restrict__ w_in, const u16* __restrict__ w_sp,
                                                    const u16* __restrict__ w_out, char* __restrict__ ws) {
  extern __shared__ __attribute__((aligned(16))) char smem[];
  DIAG_BEGIN();
  const int b = blockIdx.x, xcd = b & 7, i = b >> 3;
  const int tid = threadIdx.x, lane = tid & 63, w = __builtin_amdgcn_readfirstlane(tid >> 6);
  unsigned pf = 0;
  if (b < 8 && w == 7) {
    unsigned long long p0, p1;
    asm volatile("s_getpc_b64 s[40:41]\n\ts_add_u32 s40, s40, k2_kernel@rel32@lo+4\n\ts_addc_u32 s41, s41, k2_kernel@rel32@hi+12\n\ts_mov_b64 %0, s[40:41]" : "=s"(p0) :: "s40", "s41", "scc");
    asm volatile("s_getpc_b64 s[40:41]\n\ts_add_u32 s40, s40, k3_kernel@rel32@lo+4\n\ts_addc_u32 s41, s41, k3_kernel@rel32@hi+12\n\ts_mov_b64 %0, s[40:41]" : "=s"(p1) :: "s40", "s41", "scc");
    const unsigned nl = (unsigned)((p1 + 4800 - p0) >> 7);
#pragma unroll
    for (int u = 0; u < 3; u++) { const unsigned li = u * 64 + lane; if (li < nl) pf += *(const unsigned*)(p0 + (size_t)li * 128); }
  }
  u32x4 wv[4];
  const int wr = 8 * (xcd >> 1) + (i & 7), qd = (xcd & 1) * 4 + (i >> 3);
  {
#pragma unroll
    for (int u = 0; u < 4; u++) {
      const int idx = u * 8 + w;
      const int rg = idx & 7, cg = idx >> 3;
      wv[u] = *(const u32x4*)(w_sp + (size_t)(64 * wr + 8 * rg + (lane >> 3)) * L + 256 * qd + 64 * cg + (lane & 7) * 8);
    }
  }
  unsigned wo[8];
  const bool do_wo = (b < 224) && (tid < 256);
  if (do_wo) {
    const int fid = 4 * b + w, n = fid & 3, s = (fid >> 2) % 28, dt = fid / 112;
    const int r = lane & 15, q = lane >> 4;
    const int d = 64 * dt + 32 * (n >> 1) + 8 * (r >> 2) + (r & 3) + 4 * (n & 1);
#pragma unroll
    for (int e = 0; e < 8; e++) wo[e] = w_out[(size_t)(32 * s + 16 * (e >> 2) + 4 * q + (e & 3)) * D + d];
  }
  const int rt = 4 * (xcd >> 1) + (i & 3), cidx = i >> 2, ct = 4 * (xcd & 1) + (cidx & 3);
  if (cidx >> 2) k1_body<1>(x, w_in, ws, smem, rt, ct, w, lane);
  else k1_body<0>(x, w_in, ws, smem, rt, ct, w, lane);
  STAMP(0,6);
#pragma unroll
  for (int u = 0; u < 4; u++) {
    const int idx = u * 8 + w, rg = idx & 7, cg = idx >> 3;
    const int row = 8 * rg + (lane >> 3), ch = lane & 7;
    const int sig = 8 * qd + 2 * cg + (ch >> 2), q = ch & 3, m = row >> 4, c = row & 15;
    *(u32x4*)(ws + OFF_WF + ((size_t)((wr * 64 + sig) * 4 + m) * 64 + q * 16 + c) * 16) = wv[u];
  }
  if (do_wo) {
    const int fid = 4 * b + w;
#pragma unroll
    for (int e = 0; e < 8; e++) asm volatile("" : "+v"(wo[e]));
    u32x4 o = {wo[0] | (wo[1] << 16), wo[2] | (wo[3] << 16), wo[4] | (wo[5] << 16), wo[6] | (wo[7] << 16)};
    *(u32x4*)(ws + OFF_WOF + ((size_t)fid * 64 + lane) * 16) = o;
  }
  STAMP(0,7);
  if (pf == 0x9e3779b9u) *(volatile unsigned*)(ws + OFF_YAT) = pf;
  DIAG_END(0);
}

constexpr int K2_LDS = 52 * 1024;

DEVI void k2_gemm(const u16* __restrict__ b_sp, char* __restrict__ ws, char* smem, int b, int w, int lane) {
  const int xcd = b & 7, i0 = b >> 3, rt = 8 * (xcd >> 1) + (i0 & 7), ct = 4 * (xcd & 1) + (i0 >> 3);
  const int c = lane & 15, q = lane >> 4;
  const char* wf = ws + OFF_WF + (size_t)rt * 64 * 4096 + lane * 16;
  const char* zf = ws + OFF_Z2F + (size_t)ct * 64 * 7168 + lane * 16;
  const char* st = ws + OFF_ST2 + (size_t)(64 * w + lane) * 64;
  int moff[4];
#pragma unroll
  for (int i = 0; i < 4; i++) moff[i] = ((i + w) & 3) * 1024;
  float* rbuf = (float*)(smem + 49152 + w * 512);
  u16* mbuf = (u16*)(smem + 49152 + w * 512 + 256);
  f32x4 acc[4][7];
#pragma unroll
  for (int i = 0; i < 4; i++)
#pragma unroll
    for (int n = 0; n < 7; n++) acc[i][n] = (f32x4){0.f, 0.f, 0.f, 0.f};
  float cacc[4] = {0.f, 0.f, 0.f, 0.f};
  u32x4 aw[4], az[7], bw[4], bz[7];
  f32x4 sv[4];
  auto loadf = [&](u32x4* fw, u32x4* fz, int sig) {
#pragma unroll
    for (int i = 0; i < 4; i++) fw[i] = *(const u32x4*)(wf + (size_t)sig * 4096 + moff[i]);
#pragma unroll
    for (int n = 0; n < 7; n++) fz[n] = *(const u32x4*)(zf + (size_t)sig * 7168 + n * 1024);
  };
  auto loadst = [&](int j) {
#pragma unroll
    for (int e = 0; e < 4; e++) sv[e] = *(const f32x4*)(st + (size_t)j * 256 * 64 + e * 16);
  };
  auto finstats = [&]() {
    const float s1 = (sv[0][0] + sv[0][2]) + (sv[1][0] + sv[1][2]) + (sv[2][0] + sv[2][2]) + (sv[3][0] + sv[3][2]);
    const float s2 = (sv[0][1] + sv[0][3]) + (sv[1][1] + sv[1][3]) + (sv[2][1] + sv[2][3]) + (sv[3][1] + sv[3][3]);
    float mb, sdb; ln_stats(s1, s2, 1.0f / FH, mb, sdb);
    rbuf[lane] = __builtin_amdgcn_rcpf(sdb);
    mbuf[lane] = (u16)(__builtin_bit_cast(unsigned, mb) >> 16);
  };
  auto compute = [&](const u32x4* fw, const u32x4* fz, int e) {
    const f32x4 r0 = *(const f32x4*)(rbuf + 32 * e + 8 * q), r1 = *(const f32x4*)(rbuf + 32 * e + 8 * q + 4);
    const u32x4 mp = *(const u32x4*)(mbuf + 32 * e + 8 * q);
#pragma unroll
    for (int i = 0; i < 4; i++) {
      u32x4 wsx;
      wsx[0] = pk2(lo2f(fw[i][0]) * r0[0], hi2f(fw[i][0]) * r0[1]);
      wsx[1] = pk2(lo2f(fw[i][1]) * r0[2], hi2f(fw[i][1]) * r0[3]);
      wsx[2] = pk2(lo2f(fw[i][2]) * r1[0], hi2f(fw[i][2]) * r1[1]);
      wsx[3] = pk2(lo2f(fw[i][3]) * r1[2], hi2f(fw[i][3]) * r1[3]);
#pragma unroll
      for (int e2 = 0; e2 < 4; e2++) cacc[i] = dot2(wsx[e2], mp[e2], cacc[i]);
#pragma unroll
      for (int n = 0; n < 7; n++) acc[i][n] = mfma16(fz[n], wsx, acc[i][n]);
    }
  };
  loadf(aw, az, 2 * w);
  loadst(0);
#pragma unroll 1
  for (int j = 0; j < 7; j++) {
    const int sig = 8 * j + 2 * w;
    loadf(bw, bz, sig + 1);
    finstats();
    loadst(j + 1);
    compute(aw, az, 0);
    loadf(aw, az, sig + 8);
    compute(bw, bz, 1);
  }
  loadf(bw, bz, 56 + 2 * w + 1);
  const int T = 4 * rt + w;
  u32x2 z1v[7];
#pragma unroll
  for (int n = 0; n < 7; n++) z1v[n] = *(const u32x2*)(ws + OFF_Z1F + ((size_t)((T * 8 + ct) * 7 + n) * 64 + lane) * 8);
  const float bsp = bf2f(b_sp[16 * T + c]);
  finstats();
  compute(aw, az, 0);
  compute(bw, bz, 1);
  float* cbuf = (float*)(smem + 49152 + 2048);
#pragma unroll
  for (int i = 0; i < 4; i++) { cacc[i] += __shfl_xor(cacc[i], 16); cacc[i] += __shfl_xor(cacc[i], 32); }
  if (q == 0) {
#pragma unroll
    for (int i = 0; i < 4; i++) cbuf[(((w + i) & 3) * 4 + i) * 16 + c] = cacc[i];
  }
#pragma unroll
  for (int i = 1; i < 4; i++)
#pragma unroll
    for (int n = 0; n < 4; n++) *(f32x4*)(smem + ((((w + i) & 3) * 3 + (i - 1)) * 4 + n) * 1024 + lane * 16) = acc[i][n];
  __syncthreads();
#pragma unroll
  for (int i = 0; i < 3; i++)
#pragma unroll
    for (int n = 0; n < 4; n++) acc[0][n] += *(const f32x4*)(smem + ((w * 3 + i) * 4 + n) * 1024 + lane * 16);
  const float cs = cbuf[(w * 4 + 0) * 16 + c] + cbuf[(w * 4 + 1) * 16 + c] + cbuf[(w * 4 + 2) * 16 + c] + cbuf[(w * 4 + 3) * 16 + c];
  __syncthreads();
#pragma unroll
  for (int i = 1; i < 4; i++)
#pragma unroll
    for (int n = 4; n < 7; n++) *(f32x4*)(smem + ((((w + i) & 3) * 3 + (i - 1)) * 4 + (n - 4)) * 1024 + lane * 16) = acc[i][n];
  __syncthreads();
#pragma unroll
  for (int i = 0; i < 3; i++)
#pragma unroll
    for (int n = 4; n < 7; n++) acc[0][n] += *(const f32x4*)(smem + ((w * 3 + i) * 4 + (n - 4)) * 1024 + lane * 16);
#pragma unroll
  for (int n = 0; n < 7; n++) {
    float g[4];
#pragma unroll
    for (int e = 0; e < 4; e++) g[e] = rbf(rbf(acc[0][n][e] - cs) + bsp);
    u32x2 o = {pk2(lo2f(z1v[n][0]) * g[0], hi2f(z1v[n][0]) * g[1]), pk2(lo2f(z1v[n][1]) * g[2], hi2f(z1v[n][1]) * g[3])};
    const int Ft = 7 * ct + n;
    *(u32x2*)(ws + OFF_HF + ((size_t)(T * 28 + (Ft >> 1)) * 64 + lane) * 16 + (Ft & 1) * 8) = o;
  }
}

constexpr int SS_P = 0, SS_U = 4096, SS_UP = 4672, SS_KD = SS_U + 2 * SS_UP, SS_KR = SS_KD + 16384, SS_KP = 4288;
static_assert(SS_KR + 4 * SS_KP <= K2_LDS, "lds");

DEVI void k2_ssm(const u16* __restrict__ Are, const u16* __restrict__ Aim, const u16* __restrict__ Bre, const u16* __restrict__ Bim,
                 const u16* __restrict__ Cre, const u16* __restrict__ Cim, const u16* __restrict__ logdt, const u16* __restrict__ Dskip,
                 char* __restrict__ ws, char* smem, int b2, int w, int lane) {
  const int ch = w >> 1, hf = w & 1, d = 2 * b2 + ch, t128 = hf * 64 + lane;
  {
    const int i = d * NS + t128;
    const float dt = rbf(expf(bf2f(logdt[d])));
    const float zr = dt * bf2f(Are[i]), zi = dt * bf2f(Aim[i]);
    const float br = dt * bf2f(Bre[i]), bi = dt * bf2f(Bim[i]);
    const float cr = bf2f(Cre[i]), ci = bf2f(Cim[i]);
    *(f32x4*)(smem + SS_P + (ch * 128 + t128) * 16) = (f32x4){zr * 1.4426950408889634f, zi * 0.15915494309189535f, cr * br - ci * bi, cr * bi + ci * br};
    char* ub = smem + SS_U + ch * SS_UP;
    const u32x4* src = (const u32x4*)(ws + OFF_XNT + (size_t)d * L * 2);
    *(u32x4*)(ub + 544 + t128 * 16) = src[t128];
    *(u32x4*)(ub + 544 + 2048 + t128 * 16) = src[128 + t128];
    if (t128 < 34) *(u32x4*)(ub + t128 * 16) = (u32x4){0u, 0u, 0u, 0u};
  }
  __syncthreads();
  {
    const int r = lane & 31, h = lane >> 5;
    const float la = (float)(r < 8 ? r : r < 12 ? 256 + 2 * (r - 8) : r < 14 ? 512 + 4 * (r - 12) : r < 16 ? 768 + 4 * (r - 14) : r < 20 ? 1024 + 256 * (r - 16) : r == 20 ? 2048 : 0);
    const float lb = (float)(8 * r);
    f32x16 kacc;
#pragma unroll
    for (int e = 0; e < 16; e++) kacc[e] = 0.f;
#pragma unroll 2
    for (int s = 0; s < 8; s++) {
      u32x4 uf, vf;
#pragma unroll
      for (int e = 0; e < 4; e++) {
        const f32x4 p = *(const f32x4*)(smem + SS_P + (ch * 128 + 64 * hf + 8 * s + 4 * h + e) * 16);
        const float ma = __builtin_amdgcn_exp2f(p[0] * la), pa = __builtin_amdgcn_fractf(p[1] * la);
        const float ca = __builtin_amdgcn_cosf(pa), sa = __builtin_amdgcn_sinf(pa);
        uf[e] = pk2(ma * (p[2] * ca - p[3] * sa), -ma * (p[2] * sa + p[3] * ca));
        const float mb = __builtin_amdgcn_exp2f(p[0] * lb), pb = __builtin_amdgcn_fractf(p[1] * lb);
        vf[e] = pk2(mb * __builtin_amdgcn_cosf(pb), mb * __builtin_amdgcn_sinf(pb));
      }
      kacc = mfma32(uf, vf, kacc);
    }
    float* kd = (float*)(smem + SS_KD + w * 4096);
#pragma unroll
    for (int e = 0; e < 16; e++) kd[((e & 3) + 8 * (e >> 2) + 4 * h) * 32 + r] = kacc[e];
  }
  __syncthreads();
  {
    const float* k0 = (const float*)(smem + SS_KD + (2 * ch) * 4096);
    const float* k1 = k0 + 1024;
    auto kval = [&](int l) -> float {
      if (l < 0 || l >= L) return 0.f;
      const int lf = (int)rbf((float)l);
      const int row = lf < 256 ? (lf & 7) : lf < 512 ? 8 + ((lf & 7) >> 1) : lf < 1024 ? 12 + 2 * ((lf >> 8) & 1) + ((lf >> 2) & 1) : lf < 2048 ? 16 + ((lf >> 8) & 3) : 20;
      const int idx = row * 32 + ((lf & 255) >> 3);
      return k0[idx] + k1[idx];
    };
    unsigned* c0 = (unsigned*)(smem + SS_KR + (2 * ch) * SS_KP);
    unsigned* c1 = (unsigned*)(smem + SS_KR + (2 * ch + 1) * SS_KP);
#pragma unroll 1
    for (int j = t128; j < 1052; j += 128) {
      const int l1 = 2080 - 2 * j;
      const float a = kval(l1), bb = kval(l1 - 1), cc = kval(l1 - 2);
      c0[j] = pk2(a, bb); c1[j] = pk2(bb, cc);
    }
  }
  __syncthreads();
  const int a = lane & 15, q = lane >> 4;
  f32x4 acc[8];
#pragma unroll
  for (int e = 0; e < 8; e++) acc[e] = (f32x4){0.f, 0.f, 0.f, 0.f};
  {
    const char* kb = smem + SS_KR + (2 * ch + (a & 1)) * SS_KP + (2088 - a - 8 * q - (a & 1)) * 2;
    const char* ub = smem + SS_U + ch * SS_UP + 560 + 32 * a - 16 * q;
    auto afrag = [&](int s) -> u32x4 { const unsigned* p = (const unsigned*)(kb - 64 * s); return (u32x4){p[0], p[1], p[2], p[3]}; };
    auto bfrag = [&](int sg) -> u32x4 { return *(const u32x4*)(ub - 64 * sg); };
    if (hf == 0) {
      const u32x4 b8 = bfrag(8);
#pragma unroll
      for (int cc = 0; cc < 8; cc++) acc[cc] = mfma16(afrag(8 * (cc + 1)), b8, acc[cc]);
    }
#pragma unroll 1
    for (int ii = 0; ii < 4; ii++) {
      const int i = 4 * hf + ii;
      u32x4 af[8], bfr[8];
#pragma unroll
      for (int p = 0; p < 8; p++) { af[p] = afrag(8 * p + i); bfr[p] = bfrag(i - 8 * p); }
#pragma unroll
      for (int cc = 0; cc < 8; cc++)
#pragma unroll
        for (int p = 0; p <= cc; p++) acc[cc] = mfma16(af[p], bfr[cc - p], acc[cc]);
    }
  }
  float* red = (float*)(smem + SS_KD);
  if (hf == 0) {
#pragma unroll
    for (int e = 0; e < 4; e++) *(f32x4*)((char*)red + ((ch * 2 + 0) * 4 + e) * 1024 + lane * 16) = acc[4 + e];
  } else {
#pragma unroll
    for (int e = 0; e < 4; e++) *(f32x4*)((char*)red + ((ch * 2 + 1) * 4 + e) * 1024 + lane * 16) = acc[e];
  }
  __syncthreads();
  const float dsk = bf2f(Dskip[d]);
#pragma unroll
  for (int e = 0; e < 4; e++) {
    const f32x4 o4 = *(const f32x4*)((char*)red + ((ch * 2 + (hf ^ 1)) * 4 + e) * 1024 + lane * 16);
    const f32x4 alo = acc[e], ahi = acc[4 + e];
    const f32x4 mine = hf ? ahi : alo;
    const int t = 256 * (4 * hf + e) + 16 * a + 4 * q;
    const u32x2 uv = *(const u32x2*)(smem + SS_U + ch * SS_UP + 544 + t * 2);
    const float y0 = rbf(mine[0] + o4[0]) + rbf(lo2f(uv[0]) * dsk), y1 = rbf(mine[1] + o4[1]) + rbf(hi2f(uv[0]) * dsk);
    const float y2 = rbf(mine[2] + o4[2]) + rbf(lo2f(uv[1]) * dsk), y3 = rbf(mine[3] + o4[3]) + rbf(hi2f(uv[1]) * dsk);
    *(u32x2*)(ws + OFF_YAT + ((size_t)d * L + t) * 2) = (u32x2){pk2(y0, y1), pk2(y2, y3)};
  }
}

extern "C" __global__ __launch_bounds__(256, 1) void k2_kernel(const u16* __restrict__ b_sp, const u16* __restrict__ Are, const u16* __restrict__ Aim,
                                                    const u16* __restrict__ Bre, const u16* __restrict__ Bim, const u16* __restrict__ Cre,
                                                    const u16* __restrict__ Cim, const u16* __restrict__ logdt, const u16* __restrict__ Dskip,
                                                    char* __restrict__ ws) {
  extern __shared__ __attribute__((aligned(16))) char smem[];
  DIAG_BEGIN();
  const int lane = threadIdx.x & 63, w = __builtin_amdgcn_readfirstlane(threadIdx.x >> 6);
  if (blockIdx.x < 256) k2_gemm(b_sp, ws, smem, blockIdx.x, w, lane);
  else k2_ssm(Are, Aim, Bre, Bim, Cre, Cim, logdt, Dskip, ws, smem, blockIdx.x - 256, w, lane);
  DIAG_END(1);
}

constexpr int K3_LDS = 48 * 1024;
extern "C" __global__ __launch_bounds__(256, 1) void k3_kernel(const u16* __restrict__ x, char* __restrict__ ws, u16* __restrict__ out) {
  extern __shared__ __attribute__((aligned(16))) char smem[];
  DIAG_BEGIN();
  const int b = blockIdx.x, xcd = b & 7, i0 = b >> 3, rt = 8 * (xcd >> 1) + (i0 & 7), dt = 4 * (xcd & 1) + (i0 >> 3);
  const int lane = threadIdx.x & 63, w = __builtin_amdgcn_readfirstlane(threadIdx.x >> 6);
  const int c = lane & 15, q = lane >> 4;
  u32x4 hf[7][4], wo[7][4];
#pragma unroll
  for (int s = 0; s < 7; s++) {
#pragma unroll
    for (int i = 0; i < 4; i++) hf[s][i] = *(const u32x4*)(ws + OFF_HF + ((size_t)((4 * rt + ((i + w) & 3)) * 28 + 7 * w + s) * 64 + lane) * 16);
#pragma unroll
    for (int n = 0; n < 4; n++) wo[s][n] = *(const u32x4*)(ws + OFF_WOF + ((size_t)((dt * 28 + 7 * w + s) * 4 + n) * 64 + lane) * 16);
  }
  const int t = 64 * rt + 16 * w + c;
  u32x4 xv[2]; unsigned ya[2][8];
#pragma unroll
  for (int P = 0; P < 2; P++) {
    const int d = 64 * dt + 32 * P + 8 * q;
    xv[P] = *(const u32x4*)(x + (size_t)t * D + d);
#pragma unroll
    for (int e = 0; e < 8; e++) ya[P][e] = *(const u16*)(ws + OFF_YAT + ((size_t)(d + e) * L + t) * 2);
  }
  __builtin_amdgcn_sched_barrier(0);
  f32x4 acc[4][4];
#pragma unroll
  for (int i = 0; i < 4; i++)
#pragma unroll
    for (int n = 0; n < 4; n++) acc[i][n] = (f32x4){0.f, 0.f, 0.f, 0.f};
#pragma unroll
  for (int s = 0; s < 7; s++)
#pragma unroll
    for (int i = 0; i < 4; i++)
#pragma unroll
      for (int n = 0; n < 4; n++) acc[i][n] = mfma16(wo[s][n], hf[s][i], acc[i][n]);
#pragma unroll
  for (int i = 1; i < 4; i++)
#pragma unroll
    for (int n = 0; n < 4; n++) *(f32x4*)(smem + ((((w + i) & 3) * 3 + (i - 1)) * 4 + n) * 1024 + lane * 16) = acc[i][n];
  __syncthreads();
#pragma unroll
  for (int i = 0; i < 3; i++)
#pragma unroll
    for (int n = 0; n < 4; n++) acc[0][n] += *(const f32x4*)(smem + ((w * 3 + i) * 4 + n) * 1024 + lane * 16);
#pragma unroll
  for (int P = 0; P < 2; P++) {
    float o[8];
#pragma unroll
    for (int e = 0; e < 8; e++) {
      const float xe = (e & 1) ? hi2f(xv[P][e >> 1]) : lo2f(xv[P][e >> 1]);
      const float bo = rbf(acc[0][2 * P + (e >> 2)][e & 3]);
      o[e] = rbf(xe + __builtin_bit_cast(float, ya[P][e] << 16)) + bo;
    }
    *(u32x4*)(out + (size_t)t * D + 64 * dt + 32 * P + 8 * q) = (u32x4){pk2(o[0], o[1]), pk2(o[2], o[3]), pk2(o[4], o[5]), pk2(o[6], o[7])};
  }
  DIAG_END(2);
}

extern "C" void kernel_launch(void* const* d_in, const int* in_sizes, int n_in, void* d_out, int out_size, void* d_ws, size_t ws_size,
                              hipStream_t stream) {
  const u16* x = (const u16*)d_in[0];
  const u16 *Are = (const u16*)d_in[1], *Aim = (const u16*)d_in[2], *Bre = (const u16*)d_in[3], *Bim = (const u16*)d_in[4];
  const u16 *Cre = (const u16*)d_in[5], *Cim = (const u16*)d_in[6], *logdt = (const u16*)d_in[7], *Dskip = (const u16*)d_in[8];
  const u16* w_in = (const u16*)d_in[10];
  const u16* w_sp = (const u16*)d_in[11];
  const u16* b_sp = (const u16*)d_in[12];
  const u16* w_out = (const u16*)d_in[13];
  char* ws = (char*)d_ws;
  (void)hipFuncSetAttribute((const void*)k1_kernel, hipFuncAttributeMaxDynamicSharedMemorySize, K1_LDS);
  k1_kernel<<<256, 512, K1_LDS, stream>>>(x, w_in, w_sp, w_out, ws);
  k2_kernel<<<512, 256, K2_LDS, stream>>>(b_sp, Are, Aim, Bre, Bim, Cre, Cim, logdt, Dskip, ws);
  k3_kernel<<<256, 256, K3_LDS, stream>>>(x, ws, (u16*)d_out);
}
```

```cpp
#include <hip/hip_runtime.h>
#include <hip/hip_bf16.h>
#include <stdint.h>

typedef unsigned short u16;
typedef __bf16 bf16x2 __attribute__((ext_vector_type(2)));
typedef __bf16 bf16x8 __attribute__((ext_vector_type(8)));
typedef short s16x4 __attribute__((ext_vector_type(4)));
typedef float f32x2 __attribute__((ext_vector_type(2)));
typedef float f32x4 __attribute__((ext_vector_type(4)));
typedef float f32x16 __attribute__((ext_vector_type(16)));
typedef unsigned u32x2 __attribute__((ext_vector_type(2)));
typedef unsigned u32x4 __attribute__((ext_vector_type(4)));
#define LAS __attribute__((address_space(3)))
#define DEVI __device__ __forceinline__

constexpr int L = 2048, D = 512, F = 1792, FH = 896, NS = 128;
constexpr size_t OFF_Z1F = 0;
constexpr size_t OFF_Z2F = OFF_Z1F + (size_t)128 * 8 * 7 * 512;
constexpr size_t OFF_ST2 = OFF_Z2F + (size_t)8 * 64 * 7 * 1024;
constexpr size_t OFF_WF = OFF_ST2 + (size_t)2048 * 8 * 8;
constexpr size_t OFF_WOF = OFF_WF + (size_t)32 * 64 * 4 * 1024;
constexpr size_t OFF_XNT = OFF_WOF + (size_t)8 * 28 * 4 * 1024;
constexpr size_t OFF_HF = OFF_XNT + (size_t)512 * 2048 * 2;
constexpr size_t OFF_YAT = OFF_HF + (size_t)128 * 28 * 1024;

#define DIAG_BEGIN()
#define DIAG_END(k)
#define STAMP(kk,i)
DEVI unsigned pk2(float a, float b) { f32x2 v = {a, b}; bf16x2 r = __builtin_convertvector(v, bf16x2); return __builtin_bit_cast(unsigned, r); }
DEVI float lo2f(unsigned u) { return __builtin_bit_cast(float, u << 16); }
DEVI float hi2f(unsigned u) { return __builtin_bit_cast(float, u & 0xffff0000u); }
DEVI float rbf(float a) { return hi2f(pk2(a, a)); }
DEVI float bf2f(u16 v) { return __builtin_bit_cast(float, ((unsigned)v) << 16); }
DEVI float dot2(unsigned a, unsigned b, float c) { return __builtin_amdgcn_fdot2_f32_bf16(__builtin_bit_cast(bf16x2, a), __builtin_bit_cast(bf16x2, b), c, false); }
DEVI f32x4 mfma16(u32x4 a, u32x4 b, f32x4 c) { return __builtin_amdgcn_mfma_f32_16x16x32_bf16(__builtin_bit_cast(bf16x8, a), __builtin_bit_cast(bf16x8, b), c, 0, 0, 0); }
DEVI f32x16 mfma32(u32x4 a, u32x4 b, f32x16 c) { return __builtin_amdgcn_mfma_f32_32x32x16_bf16(__builtin_bit_cast(bf16x8, a), __builtin_bit_cast(bf16x8, b), c, 0, 0, 0); }
DEVI float gelu_tanh(float x) {
  float e = __builtin_amdgcn_exp2f(x * (-2.3022082f + -0.10294324f * x * x));
  return x * __builtin_amdgcn_rcpf(1.0f + e);
}
template <int CTRL> DEVI float dpp_mov(float v) { return __builtin_bit_cast(float, __builtin_amdgcn_update_dpp(0, __builtin_bit_cast(int, v), CTRL, 0xf, 0xf, false)); }
DEVI float row16_sum(float v) {
  v += dpp_mov<0x128>(v); v += dpp_mov<0x124>(v); v += dpp_mov<0x122>(v); v += dpp_mov<0x121>(v); return v;
}
DEVI void ln_stats(float s, float ss, float inv_n, float& mb, float& sdb) {
  float m = s * inv_n; float v = fmaxf(ss * inv_n - m * m, 0.0f);
  mb = rbf(m); sdb = rbf(__builtin_amdgcn_sqrtf(rbf(rbf(v) + 1e-5f)));
}
#define WAIT_VM(n) asm volatile("s_waitcnt vmcnt(" #n ")" ::: "memory")
#define WAIT_LGKM0() asm volatile("s_waitcnt lgkmcnt(0)" ::: "memory")
#define BAR() __builtin_amdgcn_s_barrier()
DEVI void glds16(const void* src, char* lds) { __builtin_amdgcn_global_load_lds((const unsigned*)src, (unsigned*)lds, 16, 0, 0); }

constexpr int K1_STAGE = 32768;
constexpr int K1_MISC = 4 * K1_STAGE;
constexpr int K1_LDS = K1_MISC + 2048   + 512   + 2048  ;

template <int HALF>
DEVI void k1_body(const u16* __restrict__ x, const u16* __restrict__ w_in, char* __restrict__ ws, char* smem, int rt, int ct, int w, int lane) {
  const int wm = w & 3, wn = w >> 2;
  const int f0 = HALF * FH + ct * 112;
  const u16* xs0 = x + (size_t)(128 * rt + 16 * w + (lane >> 3)) * D + (((lane & 7) ^ ((lane >> 3) & 3)) * 8);
  const u16* xs1 = x + (size_t)(128 * rt + 16 * w + 8 + (lane >> 3)) * D + (((lane & 7) ^ (((lane >> 3) & 3) | 4)) * 8);
  const int wpair = ((lane & 15) >> 1) ^ (((lane >> 4) & 3) | ((w & 1) << 2));
  const bool wact = wpair < 7;
  const u16* wsrc = w_in + (size_t)(8 * w + (lane >> 4)) * F + f0 + wpair * 16 + (lane & 1) * 8;
  auto stage = [&](int j) {
    char* sb = smem + (j & 3) * K1_STAGE;
    glds16(xs0 + 64 * j, sb + (2 * w) * 1024);
    glds16(xs1 + 64 * j, sb + (2 * w + 1) * 1024);
    if (wact) {
      glds16(wsrc + (size_t)(64 * j) * F, sb + 16384 + (2 * w) * 1024);
      glds16(wsrc + (size_t)(64 * j + 4) * F, sb + 16384 + (2 * w + 1) * 1024);
    }
  };
  STAMP(0,0);
  stage(0); stage(1); stage(2); stage(3);
  STAMP(0,1);
  const int r = lane & 15, q = lane >> 4;
  int xoff[2][2];
#pragma unroll
  for (int i = 0; i < 2; i++) {
    const int m = i ^ wn;
    const int row = 32 * wm + (HALF ? (8 * (r >> 2) + (r & 3) + 4 * m) : (16 * m + r));
    const int s = (row & 3) | (((row >> 3) & 1) << 2);
    xoff[i][0] = row * 128 + ((q ^ s) * 16);
    xoff[i][1] = row * 128 + (((4 + q) ^ s) * 16);
  }
  int woff[4];
  {
    const int qq = (lane & 15) >> 2, p = lane & 3, swz = qq | ((q & 1) << 2);
#pragma unroll
    for (int i = 0; i < 4; i++) {
      const int n = 4 * wn + ((i + wm) & 3);
      woff[i] = 16384 + (8 * q + qq) * 256 + ((n ^ swz) * 32) + 8 * p;
    }
  }
  f32x4 acc[2][4];
#pragma unroll
  for (int i = 0; i < 2; i++)
#pragma unroll
    for (int n = 0; n < 4; n++) acc[i][n] = (f32x4){0.f, 0.f, 0.f, 0.f};
  float ssum = 0.f, ssq = 0.f, cwa = 0.f;
  const unsigned ONES = 0x3f803f80u;
#pragma unroll 1
  for (int j = 0; j < 8; j++) {
    if (j == 0) WAIT_VM(12); else if (j < 6) WAIT_VM(8); else if (j == 6) WAIT_VM(4); else WAIT_VM(0);
    BAR();
    if (j == 0) STAMP(0,2); if (j == 4) STAMP(0,3);
    if (j >= 1 && j <= 4) stage(j + 3);
    const char* sb = smem + (j & 3) * K1_STAGE;
#pragma unroll
    for (int kk = 0; kk < 2; kk++) {
      u32x4 xf[2], wf[4];
#pragma unroll
      for (int i = 0; i < 2; i++) xf[i] = *(const u32x4*)(sb + xoff[i][kk]);
#pragma unroll
      for (int i = 0; i < 4; i++) {
        s16x4 a = __builtin_amdgcn_ds_read_tr16_b64_v4i16((LAS s16x4*)(sb + woff[i] + kk * 8192));
        s16x4 b = __builtin_amdgcn_ds_read_tr16_b64_v4i16((LAS s16x4*)(sb + woff[i] + kk * 8192 + 1024));
        u32x2 a2 = __builtin_bit_cast(u32x2, a), b2 = __builtin_bit_cast(u32x2, b);
        wf[i] = (u32x4){a2[0], a2[1], b2[0], b2[1]};
      }
#pragma unroll
      for (int e = 0; e < 4; e++) { ssum = dot2(xf[0][e], ONES, ssum); ssq = dot2(xf[0][e], xf[0][e], ssq); cwa = dot2(wf[0][e], ONES, cwa); }
#pragma unroll
      for (int i = 0; i < 2; i++)
#pragma unroll
        for (int n = 0; n < 4; n++) acc[i][n] = HALF ? mfma16(xf[i], wf[n], acc[i][n]) : mfma16(wf[n], xf[i], acc[i][n]);
    }
  }
  STAMP(0,4);
  f32x4* rstat = (f32x4*)(smem + K1_MISC);
  float* cwt = (float*)(smem + K1_MISC + 2048);
  f32x2* part = (f32x2*)(smem + K1_MISC + 2560);
  ssum += __shfl_xor(ssum, 16); ssum += __shfl_xor(ssum, 32);
  ssq += __shfl_xor(ssq, 16); ssq += __shfl_xor(ssq, 32);
  cwa += __shfl_xor(cwa, 16); cwa += __shfl_xor(cwa, 32);
  if (q == 0) {
    const int row = 32 * wm + (HALF ? (8 * (r >> 2) + (r & 3) + 4 * wn) : (16 * wn + r));
    float mb, sdb; ln_stats(ssum, ssq, 1.0f / D, mb, sdb);
    const float ri = 1.0f / sdb;
    if ((w & 3) == wm) rstat[row] = (f32x4){ri, ri * mb, mb, sdb};
    cwt[16 * (4 * wn + wm) + r] = cwa;
  }
  __syncthreads();
  if (HALF == 0) {
    char* z1 = ws + OFF_Z1F;
#pragma unroll
    for (int i = 0; i < 2; i++) {
      const int m = i ^ wn;
      const f32x4 rs = rstat[32 * wm + 16 * m + r];
      const int T = 8 * rt + 2 * wm + m;
#pragma unroll
      for (int n = 0; n < 4; n++) {
        const int nt = 4 * wn + ((n + wm) & 3);
        if (nt < 7) {
          const f32x4 cw = *(const f32x4*)(cwt + 16 * nt + 4 * q);
          float v[4];
#pragma unroll
          for (int e = 0; e < 4; e++) v[e] = gelu_tanh(rbf(rs[0] * acc[i][n][e] - rs[1] * cw[e]));
          u32x2 o = {pk2(v[0], v[1]), pk2(v[2], v[3])};
          *(u32x2*)(z1 + ((size_t)((T * 8 + ct) * 7 + nt) * 64 + lane) * 8) = o;
        }
      }
    }
  } else {
    char* z2 = ws + OFF_Z2F;
    float s1[8], s2[8];
#pragma unroll
    for (int e = 0; e < 8; e++) { s1[e] = 0.f; s2[e] = 0.f; }
    f32x2 rs[8];
#pragma unroll
    for (int e = 0; e < 8; e++) { const f32x4 t4 = rstat[32 * wm + 8 * q + e]; rs[e] = (f32x2){t4[0], t4[1]}; }
    const int sig = 4 * rt + wm;
#pragma unroll
    for (int n = 0; n < 4; n++) {
      const int nt = 4 * wn + ((n + wm) & 3);
      if (nt < 7) {
        const float cw = cwt[16 * nt + r];
        unsigned o[4];
#pragma unroll
        for (int m = 0; m < 2; m++) {
          const f32x4 aa = acc[m][n], ab = acc[m ^ 1][n];
          f32x4 a = wn ? ab : aa;
          float v[4];
#pragma unroll
          for (int e = 0; e < 4; e++) v[e] = gelu_tanh(rbf(rs[4 * m + e][0] * a[e] - rs[4 * m + e][1] * cw));
          o[2 * m] = pk2(v[0], v[1]); o[2 * m + 1] = pk2(v[2], v[3]);
          float z0 = lo2f(o[2 * m]), z1v = hi2f(o[2 * m]), z2v = lo2f(o[2 * m + 1]), z3 = hi2f(o[2 * m + 1]);
          s1[4 * m + 0] += z0; s2[4 * m + 0] += z0 * z0;
          s1[4 * m + 1] += z1v; s2[4 * m + 1] += z1v * z1v;
          s1[4 * m + 2] += z2v; s2[4 * m + 2] += z2v * z2v;
          s1[4 * m + 3] += z3; s2[4 * m + 3] += z3 * z3;
        }
        *(u32x4*)(z2 + ((size_t)((ct * 64 + sig) * 7 + nt) * 64 + lane) * 16) = (u32x4){o[0], o[1], o[2], o[3]};
      }
    }
#pragma unroll
    for (int e = 0; e < 8; e++) { s1[e] = row16_sum(s1[e]); s2[e] = row16_sum(s2[e]); }
    if (r == 0) {
#pragma unroll
      for (int e = 0; e < 8; e++) part[wn * 128 + 32 * wm + 8 * q + e] = (f32x2){s1[e], s2[e]};
    }
    __syncthreads();
    const int tid = w * 64 + lane;
    if (tid < 128) {
      const f32x2 a = part[tid], b = part[128 + tid];
      *(f32x2*)(ws + OFF_ST2 + ((size_t)(128 * rt + tid) * 8 + ct) * 8) = (f32x2){a[0] + b[0], a[1] + b[1]};
    }
  }
  STAMP(0,5);
  {
    const int tid = w * 64 + lane, c16 = HALF * 8 + ct;
    const int row = tid >> 2, ch8 = (tid & 3) * 8;
    const u32x4 xv = *(const u32x4*)(x + (size_t)(128 * rt + row) * D + 32 * c16 + ch8);
    const f32x4 rs = rstat[row];
    u16* tl = (u16*)smem;
#pragma unroll
    for (int e = 0; e < 4; e++) {
      const float a = rbf(lo2f(xv[e]) - rs[2]) * rs[0], b = rbf(hi2f(xv[e]) - rs[2]) * rs[0];
      const unsigned p = pk2(a, b);
      tl[(ch8 + 2 * e) * 136 + row] = (u16)p; tl[(ch8 + 2 * e + 1) * 136 + row] = (u16)(p >> 16);
    }
    __syncthreads();
    const int ch = tid >> 4, pc = tid & 15;
    const u32x4 o = *(const u32x4*)(tl + ch * 136 + pc * 8);
    *(u32x4*)(ws + OFF_XNT + ((size_t)(32 * c16 + ch) * L + 128 * rt + pc * 8) * 2) = o;
  }
}

__global__ __launch_bounds__(512, 2) void k1_kernel(const u16* __restrict__ x, const u16* __restrict__ w_in, const u16* __restrict__ w_sp,
                                                    const u16* __restrict__ w_out, char* __restrict__ ws) {
  extern __shared__ __attribute__((aligned(16))) char smem[];
  DIAG_BEGIN();
  const int b = blockIdx.x, xcd = b & 7, i = b >> 3;
  const int tid = threadIdx.x, lane = tid & 63, w = __builtin_amdgcn_readfirstlane(tid >> 6);
  u32x4 wv[4];
  const int wr = 8 * (xcd >> 1) + (i & 7), qd = (xcd & 1) * 4 + (i >> 3);
  {
#pragma unroll
    for (int u = 0; u < 4; u++) {
      const int idx = u * 8 + w;
      const int rg = idx & 7, cg = idx >> 3;
      wv[u] = *(const u32x4*)(w_sp + (size_t)(64 * wr + 8 * rg + (lane >> 3)) * L + 256 * qd + 64 * cg + (lane & 7) * 8);
    }
  }
  unsigned wo[8];
  const bool do_wo = (b < 224) && (tid < 256);
  if (do_wo) {
    const int fid = 4 * b + w, n = fid & 3, s = (fid >> 2) % 28, dt = fid / 112;
    const int r = lane & 15, q = lane >> 4;
    const int d = 64 * dt + 32 * (n >> 1) + 8 * (r >> 2) + (r & 3) + 4 * (n & 1);
#pragma unroll
    for (int e = 0; e < 8; e++) wo[e] = w_out[(size_t)(32 * s + 16 * (e >> 2) + 4 * q + (e & 3)) * D + d];
  }
  const int rt = 4 * (xcd >> 1) + (i & 3), cidx = i >> 2, ct = 4 * (xcd & 1) + (cidx & 3);
  if (cidx >> 2) k1_body<1>(x, w_in, ws, smem, rt, ct, w, lane);
  else k1_body<0>(x, w_in, ws, smem, rt, ct, w, lane);
  STAMP(0,6);
#pragma unroll
  for (int u = 0; u < 4; u++) {
    const int idx = u * 8 + w, rg = idx & 7, cg = idx >> 3;
    const int row = 8 * rg + (lane >> 3), ch = lane & 7;
    const int sig = 8 * qd + 2 * cg + (ch >> 2), q = ch & 3, m = row >> 4, c = row & 15;
    *(u32x4*)(ws + OFF_WF + ((size_t)((wr * 64 + sig) * 4 + m) * 64 + q * 16 + c) * 16) = wv[u];
  }
  if (do_wo) {
    const int fid = 4 * b + w;
#pragma unroll
    for (int e = 0; e < 8; e++) asm volatile("" : "+v"(wo[e]));
    u32x4 o = {wo[0] | (wo[1] << 16), wo[2] | (wo[3] << 16), wo[4] | (wo[5] << 16), wo[6] | (wo[7] << 16)};
    *(u32x4*)(ws + OFF_WOF + ((size_t)fid * 64 + lane) * 16) = o;
  }
  STAMP(0,7);
  DIAG_END(0);
}

constexpr int K2_LDS = 52 * 1024;

DEVI void k2_gemm(const u16* __restrict__ b_sp, char* __restrict__ ws, char* smem, int b, int w, int lane) {
  const int xcd = b & 7, i0 = b >> 3, rt = 8 * (xcd >> 1) + (i0 & 7), ct = 4 * (xcd & 1) + (i0 >> 3);
  const int c = lane & 15, q = lane >> 4;
  const char* wf = ws + OFF_WF + (size_t)rt * 64 * 4096 + lane * 16;
  const char* zf = ws + OFF_Z2F + (size_t)ct * 64 * 7168 + lane * 16;
  const char* st = ws + OFF_ST2 + (size_t)(64 * w + lane) * 64;
  int moff[4];
#pragma unroll
  for (int i = 0; i < 4; i++) moff[i] = ((i + w) & 3) * 1024;
  float* rbuf = (float*)(smem + 49152 + w * 512);
  u16* mbuf = (u16*)(smem + 49152 + w * 512 + 256);
  f32x4 acc[4][7];
#pragma unroll
  for (int i = 0; i < 4; i++)
#pragma unroll
    for (int n = 0; n < 7; n++) acc[i][n] = (f32x4){0.f, 0.f, 0.f, 0.f};
  float cacc[4] = {0.f, 0.f, 0.f, 0.f};
  u32x4 aw[4], az[7], bw[4], bz[7];
  f32x4 sv[4];
  auto loadf = [&](u32x4* fw, u32x4* fz, int sig) {
#pragma unroll
    for (int i = 0; i < 4; i++) fw[i] = *(const u32x4*)(wf + (size_t)sig * 4096 + moff[i]);
#pragma unroll
    for (int n = 0; n < 7; n++) fz[n] = *(const u32x4*)(zf + (size_t)sig * 7168 + n * 1024);
  };
  auto loadst = [&](int j) {
#pragma unroll
    for (int e = 0; e < 4; e++) sv[e] = *(const f32x4*)(st + (size_t)j * 256 * 64 + e * 16);
  };
  auto finstats = [&]() {
    const float s1 = (sv[0][0] + sv[0][2]) + (sv[1][0] + sv[1][2]) + (sv[2][0] + sv[2][2]) + (sv[3][0] + sv[3][2]);
    const float s2 = (sv[0][1] + sv[0][3]) + (sv[1][1] + sv[1][3]) + (sv[2][1] + sv[2][3]) + (sv[3][1] + sv[3][3]);
    float mb, sdb; ln_stats(s1, s2, 1.0f / FH, mb, sdb);
    rbuf[lane] = __builtin_amdgcn_rcpf(sdb);
    mbuf[lane] = (u16)(__builtin_bit_cast(unsigned, mb) >> 16);
  };
  auto compute = [&](const u32x4* fw, const u32x4* fz, int e) {
    const f32x4 r0 = *(const f32x4*)(rbuf + 32 * e + 8 * q), r1 = *(const f32x4*)(rbuf + 32 * e + 8 * q + 4);
    const u32x4 mp = *(const u32x4*)(mbuf + 32 * e + 8 * q);
#pragma unroll
    for (int i = 0; i < 4; i++) {
      u32x4 wsx;
      wsx[0] = pk2(lo2f(fw[i][0]) * r0[0], hi2f(fw[i][0]) * r0[1]);
      wsx[1] = pk2(lo2f(fw[i][1]) * r0[2], hi2f(fw[i][1]) * r0[3]);
      wsx[2] = pk2(lo2f(fw[i][2]) * r1[0], hi2f(fw[i][2]) * r1[1]);
      wsx[3] = pk2(lo2f(fw[i][3]) * r1[2], hi2f(fw[i][3]) * r1[3]);
#pragma unroll
      for (int e2 = 0; e2 < 4; e2++) cacc[i] = dot2(wsx[e2], mp[e2], cacc[i]);
#pragma unroll
      for (int n = 0; n < 7; n++) acc[i][n] = mfma16(fz[n], wsx, acc[i][n]);
    }
  };
  loadf(aw, az, 2 * w);
  loadst(0);
#pragma unroll 1
  for (int j = 0; j < 7; j++) {
    const int sig = 8 * j + 2 * w;
    loadf(bw, bz, sig + 1);
    finstats();
    loadst(j + 1);
    compute(aw, az, 0);
    loadf(aw, az, sig + 8);
    compute(bw, bz, 1);
  }
  loadf(bw, bz, 56 + 2 * w + 1);
  const int T = 4 * rt + w;
  u32x2 z1v[7];
#pragma unroll
  for (int n = 0; n < 7; n++) z1v[n] = *(const u32x2*)(ws + OFF_Z1F + ((size_t)((T * 8 + ct) * 7 + n) * 64 + lane) * 8);
  const float bsp = bf2f(b_sp[16 * T + c]);
  finstats();
  compute(aw, az, 0);
  compute(bw, bz, 1);
  float* cbuf = (float*)(smem + 49152 + 2048);
#pragma unroll
  for (int i = 0; i < 4; i++) { cacc[i] += __shfl_xor(cacc[i], 16); cacc[i] += __shfl_xor(cacc[i], 32); }
  if (q == 0) {
#pragma unroll
    for (int i = 0; i < 4; i++) cbuf[(((w + i) & 3) * 4 + i) * 16 + c] = cacc[i];
  }
#pragma unroll
  for (int i = 1; i < 4; i++)
#pragma unroll
    for (int n = 0; n < 4; n++) *(f32x4*)(smem + ((((w + i) & 3) * 3 + (i - 1)) * 4 + n) * 1024 + lane * 16) = acc[i][n];
  __syncthreads();
#pragma unroll
  for (int i = 0; i < 3; i++)
#pragma unroll
    for (int n = 0; n < 4; n++) acc[0][n] += *(const f32x4*)(smem + ((w * 3 + i) * 4 + n) * 1024 + lane * 16);
  const float cs = cbuf[(w * 4 + 0) * 16 + c] + cbuf[(w * 4 + 1) * 16 + c] + cbuf[(w * 4 + 2) * 16 + c] + cbuf[(w * 4 + 3) * 16 + c];
  __syncthreads();
#pragma unroll
  for (int i = 1; i < 4; i++)
#pragma unroll
    for (int n = 4; n < 7; n++) *(f32x4*)(smem + ((((w + i) & 3) * 3 + (i - 1)) * 4 + (n - 4)) * 1024 + lane * 16) = acc[i][n];
  __syncthreads();
#pragma unroll
  for (int i = 0; i < 3; i++)
#pragma unroll
    for (int n = 4; n < 7; n++) acc[0][n] += *(const f32x4*)(smem + ((w * 3 + i) * 4 + (n - 4)) * 1024 + lane * 16);
#pragma unroll
  for (int n = 0; n < 7; n++) {
    float g[4];
#pragma unroll
    for (int e = 0; e < 4; e++) g[e] = rbf(rbf(acc[0][n][e] - cs) + bsp);
    u32x2 o = {pk2(lo2f(z1v[n][0]) * g[0], hi2f(z1v[n][0]) * g[1]), pk2(lo2f(z1v[n][1]) * g[2], hi2f(z1v[n][1]) * g[3])};
    const int Ft = 7 * ct + n;
    *(u32x2*)(ws + OFF_HF + ((size_t)(T * 28 + (Ft >> 1)) * 64 + lane) * 16 + (Ft & 1) * 8) = o;
  }
}

constexpr int SS_P = 0, SS_U = 4096, SS_UP = 4672, SS_KD = SS_U + 2 * SS_UP, SS_KR = SS_KD + 16384, SS_KP = 4288;
static_assert(SS_KR + 4 * SS_KP <= K2_LDS, "lds");

DEVI void k2_ssm(const u16* __restrict__ Are, const u16* __restrict__ Aim, const u16* __restrict__ Bre, const u16* __restrict__ Bim,
                 const u16* __restrict__ Cre, const u16* __restrict__ Cim, const u16* __restrict__ logdt, const u16* __restrict__ Dskip,
                 char* __restrict__ ws, char* smem, int b2, int w, int lane) {
  const int ch = w >> 1, hf = w & 1, d = 2 * b2 + ch, t128 = hf * 64 + lane;
  {
    const int i = d * NS + t128;
    const float dt = rbf(expf(bf2f(logdt[d])));
    const float zr = dt * bf2f(Are[i]), zi = dt * bf2f(Aim[i]);
    const float br = dt * bf2f(Bre[i]), bi = dt * bf2f(Bim[i]);
    const float cr = bf2f(Cre[i]), ci = bf2f(Cim[i]);
    *(f32x4*)(smem + SS_P + (ch * 128 + t128) * 16) = (f32x4){zr * 1.4426950408889634f, zi * 0.15915494309189535f, cr * br - ci * bi, cr * bi + ci * br};
    char* ub = smem + SS_U + ch * SS_UP;
    const u32x4* src = (const u32x4*)(ws + OFF_XNT + (size_t)d * L * 2);
    *(u32x4*)(ub + 544 + t128 * 16) = src[t128];
    *(u32x4*)(ub + 544 + 2048 + t128 * 16) = src[128 + t128];
    if (t128 < 34) *(u32x4*)(ub + t128 * 16) = (u32x4){0u, 0u, 0u, 0u};
  }
  __syncthreads();
  {
    const int r = lane & 31, h = lane >> 5;
    const float la = (float)(r < 8 ? r : r < 12 ? 256 + 2 * (r - 8) : r < 14 ? 512 + 4 * (r - 12) : r < 16 ? 768 + 4 * (r - 14) : r < 20 ? 1024 + 256 * (r - 16) : r == 20 ? 2048 : 0);
    const float lb = (float)(8 * r);
    f32x16 kacc;
#pragma unroll
    for (int e = 0; e < 16; e++) kacc[e] = 0.f;
#pragma unroll 2
    for (int s = 0; s < 8; s++) {
      u32x4 uf, vf;
#pragma unroll
      for (int e = 0; e < 4; e++) {
        const f32x4 p = *(const f32x4*)(smem + SS_P + (ch * 128 + 64 * hf + 8 * s + 4 * h + e) * 16);
        const float ma = __builtin_amdgcn_exp2f(p[0] * la), pa = __builtin_amdgcn_fractf(p[1] * la);
        const float ca = __builtin_amdgcn_cosf(pa), sa = __builtin_amdgcn_sinf(pa);
        uf[e] = pk2(ma * (p[2] * ca - p[3] * sa), -ma * (p[2] * sa + p[3] * ca));
        const float mb = __builtin_amdgcn_exp2f(p[0] * lb), pb = __builtin_amdgcn_fractf(p[1] * lb);
        vf[e] = pk2(mb * __builtin_amdgcn_cosf(pb), mb * __builtin_amdgcn_sinf(pb));
      }
      kacc = mfma32(uf, vf, kacc);
    }
    float* kd = (float*)(smem + SS_KD + w * 4096);
#pragma unroll
    for (int e = 0; e < 16; e++) kd[((e & 3) + 8 * (e >> 2) + 4 * h) * 32 + r] = kacc[e];
  }
  __syncthreads();
  {
    const float* k0 = (const float*)(smem + SS_KD + (2 * ch) * 4096);
    const float* k1 = k0 + 1024;
    auto kval = [&](int l) -> float {
      if (l < 0 || l >= L) return 0.f;
      const int lf = (int)rbf((float)l);
      const int row = lf < 256 ? (lf & 7) : lf < 512 ? 8 + ((lf & 7) >> 1) : lf < 1024 ? 12 + 2 * ((lf >> 8) & 1) + ((lf >> 2) & 1) : lf < 2048 ? 16 + ((lf >> 8) & 3) : 20;
      const int idx = row * 32 + ((lf & 255) >> 3);
      return k0[idx] + k1[idx];
    };
    unsigned* c0 = (unsigned*)(smem + SS_KR + (2 * ch) * SS_KP);
    unsigned* c1 = (unsigned*)(smem + SS_KR + (2 * ch + 1) * SS_KP);
#pragma unroll 1
    for (int j = t128; j < 1052; j += 128) {
      const int l1 = 2080 - 2 * j;
      const float a = kval(l1), bb = kval(l1 - 1), cc = kval(l1 - 2);
      c0[j] = pk2(a, bb); c1[j] = pk2(bb, cc);
    }
  }
  __syncthreads();
  const int a = lane & 15, q = lane >> 4;
  f32x4 acc[8];
#pragma unroll
  for (int e = 0; e < 8; e++) acc[e] = (f32x4){0.f, 0.f, 0.f, 0.f};
  {
    const char* kb = smem + SS_KR + (2 * ch + (a & 1)) * SS_KP + (2088 - a - 8 * q - (a & 1)) * 2;
    const char* ub = smem + SS_U + ch * SS_UP + 560 + 32 * a - 16 * q;
    auto afrag = [&](int s) -> u32x4 { const unsigned* p = (const unsigned*)(kb - 64 * s); return (u32x4){p[0], p[1], p[2], p[3]}; };
    auto bfrag = [&](int sg) -> u32x4 { return *(const u32x4*)(ub - 64 * sg); };
    if (hf == 0) {
      const u32x4 b8 = bfrag(8);
#pragma unroll
      for (int cc = 0; cc < 8; cc++) acc[cc] = mfma16(afrag(8 * (cc + 1)), b8, acc[cc]);
    }
#pragma unroll 1
    for (int ii = 0; ii < 4; ii++) {
      const int i = 4 * hf + ii;
      u32x4 af[8], bfr[8];
#pragma unroll
      for (int p = 0; p < 8; p++) { af[p] = afrag(8 * p + i); bfr[p] = bfrag(i - 8 * p); }
#pragma unroll
      for (int cc = 0; cc < 8; cc++)
#pragma unroll
        for (int p = 0; p <= cc; p++) acc[cc] = mfma16(af[p], bfr[cc - p], acc[cc]);
    }
  }
  float* red = (float*)(smem + SS_KD);
  if (hf == 0) {
#pragma unroll
    for (int e = 0; e < 4; e++) *(f32x4*)((char*)red + ((ch * 2 + 0) * 4 + e) * 1024 + lane * 16) = acc[4 + e];
  } else {
#pragma unroll
    for (int e = 0; e < 4; e++) *(f32x4*)((char*)red + ((ch * 2 + 1) * 4 + e) * 1024 + lane * 16) = acc[e];
  }
  __syncthreads();
  const float dsk = bf2f(Dskip[d]);
#pragma unroll
  for (int e = 0; e < 4; e++) {
    const f32x4 o4 = *(const f32x4*)((char*)red + ((ch * 2 + (hf ^ 1)) * 4 + e) * 1024 + lane * 16);
    const f32x4 alo = acc[e], ahi = acc[4 + e];
    const f32x4 mine = hf ? ahi : alo;
    const int t = 256 * (4 * hf + e) + 16 * a + 4 * q;
    const u32x2 uv = *(const u32x2*)(smem + SS_U + ch * SS_UP + 544 + t * 2);
    const float y0 = rbf(mine[0] + o4[0]) + rbf(lo2f(uv[0]) * dsk), y1 = rbf(mine[1] + o4[1]) + rbf(hi2f(uv[0]) * dsk);
    const float y2 = rbf(mine[2] + o4[2]) + rbf(lo2f(uv[1]) * dsk), y3 = rbf(mine[3] + o4[3]) + rbf(hi2f(uv[1]) * dsk);
    *(u32x2*)(ws + OFF_YAT + ((size_t)d * L + t) * 2) = (u32x2){pk2(y0, y1), pk2(y2, y3)};
  }
}

__global__ __launch_bounds__(256, 1) void k2_kernel(const u16* __restrict__ b_sp, const u16* __restrict__ Are, const u16* __restrict__ Aim,
                                                    const u16* __restrict__ Bre, const u16* __restrict__ Bim, const u16* __restrict__ Cre,
                                                    const u16* __restrict__ Cim, const u16* __restrict__ logdt, const u16* __restrict__ Dskip,
                                                    char* __restrict__ ws) {
  extern __shared__ __attribute__((aligned(16))) char smem[];
  DIAG_BEGIN();
  const int lane = threadIdx.x & 63, w = __builtin_amdgcn_readfirstlane(threadIdx.x >> 6);
  if (blockIdx.x < 256) k2_gemm(b_sp, ws, smem, blockIdx.x, w, lane);
  else k2_ssm(Are, Aim, Bre, Bim, Cre, Cim, logdt, Dskip, ws, smem, blockIdx.x - 256, w, lane);
  DIAG_END(1);
}

constexpr int K3_LDS = 48 * 1024;
__global__ __launch_bounds__(256, 1) void k3_kernel(const u16* __restrict__ x, char* __restrict__ ws, u16* __restrict__ out) {
  extern __shared__ __attribute__((aligned(16))) char smem[];
  DIAG_BEGIN();
  const int b = blockIdx.x, xcd = b & 7, i0 = b >> 3, rt = 8 * (xcd >> 1) + (i0 & 7), dt = 4 * (xcd & 1) + (i0 >> 3);
  const int lane = threadIdx.x & 63, w = __builtin_amdgcn_readfirstlane(threadIdx.x >> 6);
  const int c = lane & 15, q = lane >> 4;
  u32x4 hf[7][4], wo[7][4];
#pragma unroll
  for (int s = 0; s < 7; s++) {
#pragma unroll
    for (int i = 0; i < 4; i++) hf[s][i] = *(const u32x4*)(ws + OFF_HF + ((size_t)((4 * rt + ((i + w) & 3)) * 28 + 7 * w + s) * 64 + lane) * 16);
#pragma unroll
    for (int n = 0; n < 4; n++) wo[s][n] = *(const u32x4*)(ws + OFF_WOF + ((size_t)((dt * 28 + 7 * w + s) * 4 + n) * 64 + lane) * 16);
  }
  const int t = 64 * rt + 16 * w + c;
  u32x4 xv[2]; unsigned ya[2][8];
#pragma unroll
  for (int P = 0; P < 2; P++) {
    const int d = 64 * dt + 32 * P + 8 * q;
    xv[P] = *(const u32x4*)(x + (size_t)t * D + d);
#pragma unroll
    for (int e = 0; e < 8; e++) ya[P][e] = *(const u16*)(ws + OFF_YAT + ((size_t)(d + e) * L + t) * 2);
  }
  __builtin_amdgcn_sched_barrier(0);
  f32x4 acc[4][4];
#pragma unroll
  for (int i = 0; i < 4; i++)
#pragma unroll
    for (int n = 0; n < 4; n++) acc[i][n] = (f32x4){0.f, 0.f, 0.f, 0.f};
#pragma unroll
  for (int s = 0; s < 7; s++)
#pragma unroll
    for (int i = 0; i < 4; i++)
#pragma unroll
      for (int n = 0; n < 4; n++) acc[i][n] = mfma16(wo[s][n], hf[s][i], acc[i][n]);
#pragma unroll
  for (int i = 1; i < 4; i++)
#pragma unroll
    for (int n = 0; n < 4; n++) *(f32x4*)(smem + ((((w + i) & 3) * 3 + (i - 1)) * 4 + n) * 1024 + lane * 16) = acc[i][n];
  __syncthreads();
#pragma unroll
  for (int i = 0; i < 3; i++)
#pragma unroll
    for (int n = 0; n < 4; n++) acc[0][n] += *(const f32x4*)(smem + ((w * 3 + i) * 4 + n) * 1024 + lane * 16);
#pragma unroll
  for (int P = 0; P < 2; P++) {
    float o[8];
#pragma unroll
    for (int e = 0; e < 8; e++) {
      const float xe = (e & 1) ? hi2f(xv[P][e >> 1]) : lo2f(xv[P][e >> 1]);
      const float bo = rbf(acc[0][2 * P + (e >> 2)][e & 3]);
      o[e] = rbf(xe + __builtin_bit_cast(float, ya[P][e] << 16)) + bo;
    }
    *(u32x4*)(out + (size_t)t * D + 64 * dt + 32 * P + 8 * q) = (u32x4){pk2(o[0], o[1]), pk2(o[2], o[3]), pk2(o[4], o[5]), pk2(o[6], o[7])};
  }
  DIAG_END(2);
}

__global__ void k_spin(char* ws){ long long t0=clock64(); while(clock64()-t0<100000){} if(ws==nullptr) *(volatile int*)ws=0; }
extern "C" void kernel_launch(void* const* d_in, const int* in_sizes, int n_in, void* d_out, int out_size, void* d_ws, size_t ws_size,
                              hipStream_t stream) {
  const u16* x = (const u16*)d_in[0];
  const u16 *Are = (const u16*)d_in[1], *Aim = (const u16*)d_in[2], *Bre = (const u16*)d_in[3], *Bim = (const u16*)d_in[4];
  const u16 *Cre = (const u16*)d_in[5], *Cim = (const u16*)d_in[6], *logdt = (const u16*)d_in[7], *Dskip = (const u16*)d_in[8];
  const u16* w_in = (const u16*)d_in[10];
  const u16* w_sp = (const u16*)d_in[11];
  const u16* b_sp = (const u16*)d_in[12];
  const u16* w_out = (const u16*)d_in[13];
  char* ws = (char*)d_ws;
  (void)hipFuncSetAttribute((const void*)k1_kernel, hipFuncAttributeMaxDynamicSharedMemorySize, K1_LDS);
  k1_kernel<<<256, 512, K1_LDS, stream>>>(x, w_in, w_sp, w_out, ws);
  k2_kernel<<<512, 256, K2_LDS, stream>>>(b_sp, Are, Aim, Bre, Bim, Cre, Cim, logdt, Dskip, ws);
  k3_kernel<<<256, 256, K3_LDS, stream>>>(x, ws, (u16*)d_out);
  k_spin<<<256,256,0,stream>>>(ws);
}
```

```cpp
#include <hip/hip_runtime.h>
#include <hip/hip_bf16.h>
#include <stdint.h>

typedef unsigned short u16;
typedef unsigned long long u64;
typedef __bf16 bf16x2 __attribute__((ext_vector_type(2)));
typedef __bf16 bf16x8 __attribute__((ext_vector_type(8)));
typedef short s16x4 __attribute__((ext_vector_type(4)));
typedef float f32x2 __attribute__((ext_vector_type(2)));
typedef float f32x4 __attribute__((ext_vector_type(4)));
typedef float f32x16 __attribute__((ext_vector_type(16)));
typedef unsigned u32x2 __attribute__((ext_vector_type(2)));
typedef unsigned u32x4 __attribute__((ext_vector_type(4)));
#define LAS __attribute__((address_space(3)))
#define GAS __attribute__((address_space(1)))
#define DEVI __device__ __forceinline__
#define RLX_AGENT __ATOMIC_RELAXED, __HIP_MEMORY_SCOPE_AGENT

constexpr int L = 2048, D = 512, F = 1792, FH = 896, NS = 128;
constexpr size_t OFF_Z2F = 0;
constexpr size_t OFF_XNT = OFF_Z2F + (size_t)8 * 64 * 7 * 1024;
constexpr size_t OFF_HF = OFF_XNT + (size_t)512 * 2048 * 2;
constexpr size_t OFF_YAT = OFF_HF + (size_t)128 * 28 * 1024;
constexpr size_t OFF_EX = OFF_YAT + (size_t)512 * 2048 * 2;
constexpr size_t OFF_FLG = OFF_EX + (size_t)32 * 8 * 64 * 16;
constexpr size_t OFF_NONCE = OFF_FLG + 3 * 256 * 128;
constexpr size_t OFF_Z1DBG = OFF_NONCE + 4096;
constexpr unsigned Z2F_BYTES = 8 * 64 * 7 * 1024, XNT_BYTES = 512 * 2048 * 2, HF_BYTES = 128 * 28 * 1024, EX_BYTES = 32 * 8 * 64 * 16;

constexpr int P1_STAGE = 40960;
constexpr int LDS_Z1 = 98304;
constexpr int LDS_SSM = 114688;
constexpr int LDS_MISC = 151552;
constexpr int LDS_SYNC = 159744;
constexpr int LDS_TOTAL = 160768;
constexpr int SS_P = LDS_SSM, SS_U = SS_P + 4096, SS_UP = 4672, SS_T = SS_U + 2 * SS_UP, SS_TP = 2592  , SS_KR = SS_T + 2 * SS_TP, SS_KP = 4288;
static_assert(SS_KR + 4 * SS_KP <= LDS_MISC, "ssm lds");
static_assert((SS_KR % 16) == 0 && (SS_T % 16) == 0, "align");

#define STAMP(i)

DEVI unsigned pk2(float a, float b) { f32x2 v = {a, b}; bf16x2 r = __builtin_convertvector(v, bf16x2); return __builtin_bit_cast(unsigned, r); }
DEVI float asf(unsigned u) { return __builtin_bit_cast(float, u); }
DEVI unsigned asu(float f) { return __builtin_bit_cast(unsigned, f); }
DEVI float lo2f(unsigned u) { return __builtin_bit_cast(float, u << 16); }
DEVI float hi2f(unsigned u) { return __builtin_bit_cast(float, u & 0xffff0000u); }
DEVI float rbf(float a) { return hi2f(pk2(a, a)); }
DEVI float bf2f(u16 v) { return __builtin_bit_cast(float, ((unsigned)v) << 16); }
DEVI float dot2(unsigned a, unsigned b, float c) { return __builtin_amdgcn_fdot2_f32_bf16(__builtin_bit_cast(bf16x2, a), __builtin_bit_cast(bf16x2, b), c, false); }
DEVI f32x4 mfma16(u32x4 a, u32x4 b, f32x4 c) { return __builtin_amdgcn_mfma_f32_16x16x32_bf16(__builtin_bit_cast(bf16x8, a), __builtin_bit_cast(bf16x8, b), c, 0, 0, 0); }
DEVI f32x16 mfma32(u32x4 a, u32x4 b, f32x16 c) { return __builtin_amdgcn_mfma_f32_32x32x16_bf16(__builtin_bit_cast(bf16x8, a), __builtin_bit_cast(bf16x8, b), c, 0, 0, 0); }
DEVI float gelu_tanh(float x) {
  const float e = __builtin_amdgcn_exp2f(x * (-2.3022082f + -0.10294324f * x * x));
  return x * __builtin_amdgcn_rcpf(1.0f + e);
}
template <int CTRL> DEVI float dpp_mov(float v) { return __builtin_bit_cast(float, __builtin_amdgcn_update_dpp(0, __builtin_bit_cast(int, v), CTRL, 0xf, 0xf, false)); }
DEVI float row16_sum(float v) { v += dpp_mov<0x128>(v); v += dpp_mov<0x124>(v); v += dpp_mov<0x122>(v); v += dpp_mov<0x121>(v); return v; }
DEVI float quad4_sum(float v) { v += __shfl_xor(v, 16); v += __shfl_xor(v, 32); return v; }
DEVI void ln_stats(float s, float ss, float inv_n, float& mb, float& sdb) {
  const float m = s * inv_n; const float v = fmaxf(ss * inv_n - m * m, 0.0f);
  mb = rbf(m); sdb = rbf(__builtin_amdgcn_sqrtf(rbf(rbf(v) + 1e-5f)));
}
#define WAIT_VM(n) asm volatile("s_waitcnt vmcnt(" #n ")" ::: "memory")
#define WAIT_LGKM0() asm volatile("s_waitcnt lgkmcnt(0)" ::: "memory")
#define BAR() __builtin_amdgcn_s_barrier()
#define LBAR() do { WAIT_LGKM0(); BAR(); } while (0)
DEVI void glds16(const void* src, char* lds) { __builtin_amdgcn_global_load_lds((const unsigned*)src, (unsigned*)lds, 16, 0, 0); }
DEVI u32x4 ldtr(const char* p0, const char* p1) {
  const u32x2 a = __builtin_bit_cast(u32x2, __builtin_amdgcn_ds_read_tr16_b64_v4i16((LAS s16x4*)p0));
  const u32x2 b = __builtin_bit_cast(u32x2, __builtin_amdgcn_ds_read_tr16_b64_v4i16((LAS s16x4*)p1));
  return (u32x4){a[0], a[1], b[0], b[1]};
}
DEVI int trow(int Tt, int c) { return 32 * (Tt >> 1) + 8 * (c >> 2) + (c & 3) + 4 * (Tt & 1); }
constexpr unsigned SPIN_CAP = 1u << 22;
DEVI void gsync(char* smem, int idx, unsigned target) {
  volatile LAS unsigned* cnt = (volatile LAS unsigned*)(smem + LDS_SYNC) + idx;
  WAIT_LGKM0();
  if ((threadIdx.x & 63) == 0) __hip_atomic_fetch_add((LAS unsigned*)cnt, 1u, __ATOMIC_RELAXED, __HIP_MEMORY_SCOPE_WORKGROUP);
  for (unsigned s = 0; s < SPIN_CAP; s++) { if ((unsigned)__builtin_amdgcn_readfirstlane(*cnt) >= target) break; __builtin_amdgcn_s_sleep(0); }
  asm volatile("" ::: "memory");
}
typedef GAS u64 gu64;
DEVI void wait_flags(char* ws, int which, int blk, bool active, u64 tag) {
  gu64* p = (gu64*)(ws + OFF_FLG + ((size_t)which * 256 + blk) * 128);
  for (unsigned s = 0; s < SPIN_CAP; s++) {
    const u64 v = active ? __hip_atomic_load(p, RLX_AGENT) : tag;
    if (__all(v == tag)) break;
    __builtin_amdgcn_s_sleep(1);
  }
  asm volatile("" ::: "memory");
}
DEVI void set_flag(char* ws, int which, int blk, u64 tag) { __hip_atomic_store((gu64*)(ws + OFF_FLG + ((size_t)which * 256 + blk) * 128), tag, RLX_AGENT); }
DEVI __amdgpu_buffer_rsrc_t mk_rsrc(const void* p, unsigned bytes) { return __builtin_amdgcn_make_buffer_rsrc((void*)p, 0, bytes, 0x00020000); }
constexpr int SC1 = 16;

struct Args {
  const u16 *x, *Are, *Aim, *Bre, *Bim, *Cre, *Cim, *logdt, *Dskip, *w_in, *w_sp, *b_sp, *w_out;
  u16* out; char* ws;
};

struct P1Frag { u32x4 xf[2], w1[2], w2[2]; };

DEVI void phase1(const Args& a, char* smem, int rt, int ct, int w, int lane, u64 tag) {
  char* ws = a.ws;
  const int wm = w & 1, wn = w >> 1;
  const int r = lane & 15, q = lane >> 4;
  const u16* xs = a.x + (size_t)(64 * rt + 8 * w + (lane >> 3)) * D + (((lane & 7) ^ (((lane >> 3) & 3) | ((w & 1) << 2))) * 8);
  const int hi = lane >> 5, s16 = lane & 31;
  const int pe = ((s16 >> 1) & 7) ^ (hi | ((w & 1) << 2)), po = ((s16 >> 1) & 7) ^ ((2 + hi) | ((w & 1) << 2));
  const bool acte = pe < 7, acto = po < 7;
  const u16* wse = a.w_in + (size_t)(8 * w + hi) * F + (s16 >> 4) * FH + 112 * ct + pe * 16 + (s16 & 1) * 8;
  const u16* wso = a.w_in + (size_t)(8 * w + 2 + hi) * F + (s16 >> 4) * FH + 112 * ct + po * 16 + (s16 & 1) * 8;
  auto stage = [&](int j, int slot) {
    char* sb = smem + slot * P1_STAGE;
    glds16(xs + 64 * j, sb + w * 1024);
    char* wb = sb + 8192 + w * 4096;
    const size_t ko = (size_t)(64 * j) * F;
    if (acte) { glds16(wse + ko, wb); glds16(wse + ko + 4 * F, wb + 2048); }
    if (acto) { glds16(wso + ko, wb + 1024); glds16(wso + ko + 4 * F, wb + 3072); }
  };
  stage(0, 0); stage(1, 1); stage(2, 2);
  const int sx = (r & 3) | (((r >> 2) & 1) << 2);
  const int xrow = 32 * wm + 8 * (r >> 2) + (r & 3);
  const int xo0 = xrow * 128 + ((q ^ sx) * 16), xo1 = xrow * 128 + (((4 + q) ^ sx) * 16);
  const int qq = (lane & 15) >> 2, p = lane & 3, swz = qq | ((q & 1) << 2);
  const int wo0 = 8192 + (8 * q + qq) * 512 + (((2 * wn) ^ swz) * 32) + 8 * p;
  const int wo1 = 8192 + (8 * q + qq) * 512 + (((2 * wn + 1) ^ swz) * 32) + 8 * p;
  auto readf = [&](P1Frag& f, const char* sb, int kk) {
    const int xo = kk ? xo1 : xo0;
    f.xf[0] = *(const u32x4*)(sb + xo); f.xf[1] = *(const u32x4*)(sb + xo + 512);
    const char* b0 = sb + wo0 + kk * 16384; const char* b1 = sb + wo1 + kk * 16384;
    f.w1[0] = ldtr(b0, b0 + 2048); f.w1[1] = ldtr(b1, b1 + 2048);
    f.w2[0] = ldtr(b0 + 256, b0 + 2304); f.w2[1] = ldtr(b1 + 256, b1 + 2304);
  };
  f32x4 acc1[2][2], acc2[2][2];
#pragma unroll
  for (int m = 0; m < 2; m++)
#pragma unroll
    for (int i = 0; i < 2; i++) { acc1[m][i] = (f32x4){0.f, 0.f, 0.f, 0.f}; acc2[m][i] = (f32x4){0.f, 0.f, 0.f, 0.f}; }
  float ssum[2] = {0.f, 0.f}, ssq[2] = {0.f, 0.f}, cwa[4] = {0.f, 0.f, 0.f, 0.f};
  const unsigned ONES = 0x3f803f80u;
  auto compute = [&](const P1Frag& f, int kk, int j) {
    if (((2 * j + kk) & 3) == wn) {
#pragma unroll
      for (int m = 0; m < 2; m++)
#pragma unroll
        for (int e = 0; e < 4; e++) { ssum[m] = dot2(f.xf[m][e], ONES, ssum[m]); ssq[m] = dot2(f.xf[m][e], f.xf[m][e], ssq[m]); }
    }
    if (kk == wm) {
#pragma unroll
      for (int e = 0; e < 4; e++) { cwa[0] = dot2(f.w1[0][e], ONES, cwa[0]); cwa[1] = dot2(f.w1[1][e], ONES, cwa[1]); cwa[2] = dot2(f.w2[0][e], ONES, cwa[2]); cwa[3] = dot2(f.w2[1][e], ONES, cwa[3]); }
    }
#pragma unroll
    for (int m = 0; m < 2; m++)
#pragma unroll
      for (int i = 0; i < 2; i++) { acc2[m][i] = mfma16(f.xf[m], f.w2[i], acc2[m][i]); acc1[m][i] = mfma16(f.w1[i], f.xf[m], acc1[m][i]); }
  };
  P1Frag fa, fb;
  WAIT_VM(10); BAR();
  readf(fa, smem, 0);
  int slot = 0;
#pragma unroll 1
  for (int j = 0; j < 8; j++) {
    const char* sb = smem + slot * P1_STAGE;
    readf(fb, sb, 1);
    compute(fa, 0, j);
    const int nslot = slot == 2 ? 0 : slot + 1;
    if (j < 7) {
      if (j < 6) WAIT_VM(5); else WAIT_VM(0);
      LBAR();
      if (j <= 4) stage(j + 3, slot);
      readf(fa, smem + nslot * P1_STAGE, 0);
    }
    compute(fb, 1, j);
    slot = nslot;
  }
  STAMP(2);
  f32x2* xpart = (f32x2*)(smem + LDS_MISC);
  float* cwpart = (float*)(smem + LDS_MISC + 2048);
  f32x4* rstat = (f32x4*)(smem + LDS_MISC + 4096);
  float* cwt = (float*)(smem + LDS_MISC + 5120);
  f32x2* part = (f32x2*)(smem + LDS_MISC + 6144);
  f32x2* st2 = (f32x2*)(smem + LDS_MISC + 2048);
#pragma unroll
  for (int m = 0; m < 2; m++) { ssum[m] = quad4_sum(ssum[m]); ssq[m] = quad4_sum(ssq[m]); }
#pragma unroll
  for (int e = 0; e < 4; e++) cwa[e] = quad4_sum(cwa[e]);
  if (q == 0) {
#pragma unroll
    for (int m = 0; m < 2; m++) xpart[wn * 64 + xrow + 4 * m] = (f32x2){ssum[m], ssq[m]};
#pragma unroll
    for (int e = 0; e < 4; e++) cwpart[((wm * 2 + (e >> 1)) * 8 + 2 * wn + (e & 1)) * 16 + r] = cwa[e];
  }
  LBAR();
  {
    const int tid = w * 64 + lane;
    if (tid < 64) {
      const f32x2 p0 = xpart[tid], p1 = xpart[64 + tid], p2 = xpart[128 + tid], p3 = xpart[192 + tid];
      float mb, sdb; ln_stats((p0[0] + p1[0]) + (p2[0] + p3[0]), (p0[1] + p1[1]) + (p2[1] + p3[1]), 1.0f / D, mb, sdb);
      const float ri = __builtin_amdgcn_rcpf(sdb);
      rstat[tid] = (f32x4){ri, ri * mb, mb, sdb};
    } else if (tid >= 256) { const int i = tid - 256; cwt[i] = cwpart[i] + cwpart[256 + i]; }
  }
  LBAR();
  float v2[2][2][4];
  {
    float s1[2][4], s2[2][4];
    const float cw0 = cwt[(8 + 2 * wn) * 16 + r], cw1 = cwt[(8 + 2 * wn + 1) * 16 + r];
#pragma unroll
    for (int m = 0; m < 2; m++)
#pragma unroll
      for (int j = 0; j < 4; j++) {
        const f32x4 rs = rstat[32 * wm + 8 * q + 4 * m + j];
        const float g0 = gelu_tanh(rs[0] * acc2[m][0][j] - rs[1] * cw0);
        float g1 = gelu_tanh(rs[0] * acc2[m][1][j] - rs[1] * cw1);
        if (wn == 3) g1 = 0.f;
        v2[m][0][j] = g0; v2[m][1][j] = g1;
        s1[m][j] = row16_sum(g0 + g1); s2[m][j] = row16_sum(g0 * g0 + g1 * g1);
      }
    if (r == 0) {
#pragma unroll
      for (int m = 0; m < 2; m++)
#pragma unroll
        for (int j = 0; j < 4; j++) part[wn * 64 + 32 * wm + 8 * q + 4 * m + j] = (f32x2){s1[m][j], s2[m][j]};
    }
  }
  LBAR();
  const __amdgpu_buffer_rsrc_t exr = mk_rsrc(ws + OFF_EX, EX_BYTES);
  if (w == 7) {
    const f32x2 p0 = part[lane], p1 = part[64 + lane], p2 = part[128 + lane], p3 = part[192 + lane];
    const u32x4 gv = {asu((p0[0] + p1[0]) + (p2[0] + p3[0])), asu((p0[1] + p1[1]) + (p2[1] + p3[1])), (unsigned)tag, (unsigned)(tag >> 32)};
    __builtin_amdgcn_raw_buffer_store_b128(gv, exr, ((rt * 8 + ct) * 64 + lane) * 16, 0, SC1);
  }
  STAMP(3);
#pragma unroll
  for (int m = 0; m < 2; m++) {
    const f32x4 rs = rstat[xrow + 4 * m];
#pragma unroll
    for (int i = 0; i < 2; i++) {
      const int n = 2 * wn + i;
      const f32x4 cw = *(const f32x4*)(cwt + n * 16 + 4 * q);
      float v[4];
#pragma unroll
      for (int e = 0; e < 4; e++) v[e] = gelu_tanh(rs[0] * acc1[m][i][e] - rs[1] * cw[e]);
      if (n < 7) *(u32x2*)(smem + LDS_Z1 + (((2 * wm + m) * 7 + n) * 64 + lane) * 8) = (u32x2){pk2(v[0], v[1]), pk2(v[2], v[3])};
    }
  }
  {
    const int tid = w * 64 + lane, row = tid >> 3, ch8 = (tid & 7) * 8;
    const u32x4 xv = *(const u32x4*)(a.x + (size_t)(64 * rt + row) * D + 64 * ct + ch8);
    const f32x4 rs = rstat[row];
    u16* tl = (u16*)(smem + LDS_SSM);
#pragma unroll
    for (int e = 0; e < 4; e++) {
      const unsigned pk = pk2(rbf(lo2f(xv[e]) - rs[2]) * rs[0], rbf(hi2f(xv[e]) - rs[2]) * rs[0]);
      tl[(ch8 + 2 * e) * 72 + row] = (u16)pk; tl[(ch8 + 2 * e + 1) * 72 + row] = (u16)(pk >> 16);
    }
    LBAR();
    const int ch = tid >> 3, pc = tid & 7;
    const u32x4 o = *(const u32x4*)(tl + ch * 72 + pc * 8);
    __builtin_amdgcn_raw_buffer_store_b128(o, mk_rsrc(ws + OFF_XNT, XNT_BYTES), ((64 * ct + ch) * L + 64 * rt + pc * 8) * 2, 0, SC1);
  }
  STAMP(4);
  if (w == 7) {
    float s1 = 0.f, s2 = 0.f;
    for (unsigned s = 0; s < SPIN_CAP; s++) {
      bool ok = true; s1 = 0.f; s2 = 0.f;
#pragma unroll
      for (int c8 = 0; c8 < 8; c8++) {
        const u32x4 g = __builtin_amdgcn_raw_buffer_load_b128(exr, ((rt * 8 + c8) * 64 + lane) * 16, 0, SC1);
        ok &= (g[2] == (unsigned)tag) & (g[3] == (unsigned)(tag >> 32));
        s1 += asf(g[0]); s2 += asf(g[1]);
      }
      if (__all(ok)) break;
      __builtin_amdgcn_s_sleep(1);
      asm volatile("" ::: "memory");
    }
    float mb, sdb; ln_stats(s1, s2, 1.0f / FH, mb, sdb);
    st2[lane] = (f32x2){mb, __builtin_amdgcn_rcpf(sdb)};
  }
  LBAR();
  STAMP(5);
  {
    const __amdgpu_buffer_rsrc_t zr = mk_rsrc(ws + OFF_Z2F, Z2F_BYTES);
    f32x2 st[8];
#pragma unroll
    for (int e = 0; e < 8; e++) st[e] = st2[32 * wm + 8 * q + e];
#pragma unroll
    for (int i = 0; i < 2; i++) {
      const int n = 2 * wn + i;
      unsigned o[4];
#pragma unroll
      for (int m = 0; m < 2; m++) {
        float z[4];
#pragma unroll
        for (int j = 0; j < 4; j++) z[j] = (rbf(v2[m][i][j]) - st[4 * m + j][0]) * st[4 * m + j][1];
        o[2 * m] = pk2(z[0], z[1]); o[2 * m + 1] = pk2(z[2], z[3]);
      }
      if (n < 7) __builtin_amdgcn_raw_buffer_store_b128((u32x4){o[0], o[1], o[2], o[3]}, zr, (((ct * 64 + 2 * rt + wm) * 7 + n) * 64 + lane) * 16, 0, SC1);
    }
  }
  WAIT_VM(0);
  LBAR();
  if (w == 0 && lane == 0) set_flag(ws, 0, blockIdx.x, tag);
  STAMP(6);
}

DEVI int blk_of(int rt, int ct) { return (((rt & 7) + 8 * (ct & 3)) << 3) | (((rt >> 3) << 1) | (ct >> 2)); }

#define ZLOAD(buf, base)                                                                                                   \
  asm volatile("global_load_dwordx4 %0, %7, %8 offset:-3072 sc1\n\tglobal_load_dwordx4 %1, %7, %8 offset:-2048 sc1\n\t"      \
               "global_load_dwordx4 %2, %7, %8 offset:-1024 sc1\n\tglobal_load_dwordx4 %3, %7, %8 sc1\n\t"                   \
               "global_load_dwordx4 %4, %7, %8 offset:1024 sc1\n\tglobal_load_dwordx4 %5, %7, %8 offset:2048 sc1\n\t"        \
               "global_load_dwordx4 %6, %7, %8 offset:3072 sc1"                                                             \
               : "=&v"(buf[0]), "=&v"(buf[1]), "=&v"(buf[2]), "=&v"(buf[3]), "=&v"(buf[4]), "=&v"(buf[5]), "=&v"(buf[6])    \
               : "v"(voff), "s"(base) : "memory")
#define ZWAIT(n, buf)                                                                                                      \
  asm volatile("s_waitcnt vmcnt(" #n ")" : "+v"(buf[0]), "+v"(buf[1]), "+v"(buf[2]), "+v"(buf[3]), "+v"(buf[4]), "+v"(buf[5]), "+v"(buf[6]) :: "memory")

DEVI void p2_wstage(const Args& a, char* smem, int rt, int w, int lane, int j, int slot) {
  const u16* src = a.w_sp + (size_t)(64 * rt + (lane >> 3)) * L + 256 * j + 64 * w;
  char* dst = smem + w * 24576 + slot * 8192;
#pragma unroll
  for (int pc = 0; pc < 8; pc++) {
    const int chunk = (lane & 7) ^ (((lane >> 3) & 3) | ((pc & 1) << 2));
    glds16(src + (size_t)(8 * pc) * L + chunk * 8, dst + pc * 1024);
  }
}

DEVI void phase2_gemm(const Args& a, char* smem, int rt, int ct, int w, int lane, u64 tag) {
  char* ws = a.ws;
  const int c = lane & 15, q = lane >> 4;
  wait_flags(ws, 0, blk_of(4 * (lane & 7) + w, ct), lane < 8, tag);
  STAMP(8);
  const unsigned voff = lane * 16;
  const char* zb = ws + OFF_Z2F + (size_t)(ct * 64 + 2 * w) * 7168 + 3072;
  u32x4 za[7], zc[7];
  ZLOAD(za, zb); ZLOAD(zc, zb + 7168);
  const int sx = (c & 3) | (((c >> 2) & 1) << 2);
  int wo[4][2];
#pragma unroll
  for (int i = 0; i < 4; i++) {
    const int row = trow((w + i) & 3, c);
    wo[i][0] = w * 24576 + row * 128 + ((q ^ sx) * 16); wo[i][1] = w * 24576 + row * 128 + (((4 + q) ^ sx) * 16);
  }
  f32x4 acc[4][7];
#pragma unroll
  for (int i = 0; i < 4; i++)
#pragma unroll
    for (int n = 0; n < 7; n++) acc[i][n] = (f32x4){0.f, 0.f, 0.f, 0.f};
  int slot = 0;
#define P2_HALF(zbuf, kk)                                                                     \
  do {                                                                                        \
    u32x4 wf[4];                                                                              \
    _Pragma("unroll") for (int i = 0; i < 4; i++) wf[i] = *(const u32x4*)(sb + wo[i][kk]);    \
    _Pragma("unroll") for (int i = 0; i < 4; i++)                                             \
      _Pragma("unroll") for (int n = 0; n < 7; n++) acc[i][n] = mfma16(zbuf[n], wf[i], acc[i][n]); \
  } while (0)
#pragma unroll 1
  for (int j = 0; j < 7; j++) {
    const char* sb = smem + slot * 8192;
    ZWAIT(7, za);
    if (j >= 1 && j <= 5) p2_wstage(a, smem, rt, w, lane, j + 2, slot == 0 ? 2 : slot - 1);
    P2_HALF(za, 0);
    const char* zn = zb + (size_t)(j + 1) * 8 * 7168;
    ZLOAD(za, zn);
    ZWAIT(7, zc);
    P2_HALF(zc, 1);
    ZLOAD(zc, zn + 7168);
    WAIT_LGKM0();
    slot = slot == 2 ? 0 : slot + 1;
  }
  {
    const char* sb = smem + slot * 8192;
    ZWAIT(0, za); ZWAIT(0, zc);
    P2_HALF(za, 0);
    P2_HALF(zc, 1);
  }
  STAMP(9);
#pragma unroll
  for (int i = 1; i < 4; i++)
#pragma unroll
    for (int n = 0; n < 7; n++) *(f32x4*)(smem + w * 24576 + ((i - 1) * 7 + n) * 1024 + lane * 16) = acc[i][n];
  const float bsp = bf2f(a.b_sp[64 * rt + trow(w, c)]);
  gsync(smem, 0, 4);
#pragma unroll
  for (int i = 1; i < 4; i++)
#pragma unroll
    for (int n = 0; n < 7; n++) acc[0][n] += *(const f32x4*)(smem + ((w - i) & 3) * 24576 + ((i - 1) * 7 + n) * 1024 + lane * 16);
  {
    const __amdgpu_buffer_rsrc_t hr = mk_rsrc(ws + OFF_HF, HF_BYTES);
    const int T = 4 * rt + w;
#pragma unroll
    for (int n = 0; n < 7; n++) {
      const u32x2 z1 = *(const u32x2*)(smem + LDS_Z1 + ((w * 7 + n) * 64 + lane) * 8);
      float g[4];
#pragma unroll
      for (int e = 0; e < 4; e++) g[e] = rbf(rbf(acc[0][n][e]) + bsp);
      const u32x2 o = {pk2(lo2f(z1[0]) * g[0], hi2f(z1[0]) * g[1]), pk2(lo2f(z1[1]) * g[2], hi2f(z1[1]) * g[3])};
      const int Ft = 7 * ct + n;
      __builtin_amdgcn_raw_buffer_store_b64(o, hr, ((T * 28 + (Ft >> 1)) * 64 + lane) * 16 + (Ft & 1) * 8, 0, SC1);
    }
  }
  WAIT_VM(0);
  gsync(smem, 0, 8);
  if (w == 0 && lane == 0) set_flag(ws, 1, blockIdx.x, tag);
  STAMP(10);
}

DEVI void ssm_kgen(const Args& a, char* smem, int b, int w4, int lane) {
  const int ch = w4 >> 1, hf = w4 & 1, d = 2 * b + ch, t128 = hf * 64 + lane;
  float* T = (float*)(smem + SS_T + ch * SS_TP);
  {
    const int i = d * NS + t128;
    const float dt = rbf(__expf(bf2f(a.logdt[d])));
    const float zr = dt * bf2f(a.Are[i]), zi = dt * bf2f(a.Aim[i]);
    const float br = dt * bf2f(a.Bre[i]), bi = dt * bf2f(a.Bim[i]);
    const float cr = bf2f(a.Cre[i]), ci = bf2f(a.Cim[i]);
    *(f32x4*)(smem + SS_P + (ch * 128 + t128) * 16) = (f32x4){zr * 1.4426950408889634f, zi * 0.15915494309189535f, cr * br - ci * bi, cr * bi + ci * br};
    for (int v = t128; v < 648; v += 128) T[v] = 0.f;
    if (t128 < 34) *(u32x4*)(smem + SS_U + ch * SS_UP + t128 * 16) = (u32x4){0u, 0u, 0u, 0u};
  }
  gsync(smem, 2 + ch, 2);
  {
    const int r = lane & 31, h = lane >> 5;
    const float la = (float)(r < 8 ? r : r < 12 ? 256 + 2 * (r - 8) : r < 14 ? 512 + 4 * (r - 12) : r < 16 ? 768 + 4 * (r - 14) : r < 20 ? 1024 + 256 * (r - 16) : r == 20 ? 2048 : 0);
    const float lb = (float)(8 * r);
    f32x16 kacc;
#pragma unroll
    for (int e = 0; e < 16; e++) kacc[e] = 0.f;
#pragma unroll 2
    for (int s = 0; s < 8; s++) {
      u32x4 uf, vf;
#pragma unroll
      for (int e = 0; e < 4; e++) {
        const f32x4 p = *(const f32x4*)(smem + SS_P + (ch * 128 + 64 * hf + 8 * s + 4 * h + e) * 16);
        const float ma = __builtin_amdgcn_exp2f(p[0] * la), pa = __builtin_amdgcn_fractf(p[1] * la);
        const float ca = __builtin_amdgcn_cosf(pa), sa = __builtin_amdgcn_sinf(pa);
        uf[e] = pk2(ma * (p[2] * ca - p[3] * sa), -ma * (p[2] * sa + p[3] * ca));
        const float mb = __builtin_amdgcn_exp2f(p[0] * lb), pb = __builtin_amdgcn_fractf(p[1] * lb);
        vf[e] = pk2(mb * __builtin_amdgcn_cosf(pb), mb * __builtin_amdgcn_sinf(pb));
      }
      kacc = mfma32(uf, vf, kacc);
    }
#pragma unroll
    for (int e = 0; e < 4; e++) {
      atomicAdd(&T[(e + 4 * h) + 8 * r], kacc[e]);
      const int v1 = h ? ((e < 2 ? 384 + e : 448 + e - 2) + 2 * r) : (256 + e + 4 * r);
      atomicAdd(&T[v1], kacc[4 + e]);
      if (h == 0) atomicAdd(&T[512 + 32 * e + r], kacc[8 + e]);
    }
    if (h == 1 && r == 0) atomicAdd(&T[640], kacc[8]);
  }
  gsync(smem, 2 + ch, 4);
  {
    unsigned* c0 = (unsigned*)(smem + SS_KR + (2 * ch) * SS_KP);
    unsigned* c1 = (unsigned*)(smem + SS_KR + (2 * ch + 1) * SS_KP);
    auto kval = [&](int l) -> float {
      const unsigned u = pk2((float)l, 0.f) & 0xffffu;
      int vi = l < 128 ? l : (int)u - 17024;
      vi = ((unsigned)l < (unsigned)L) ? vi : 641;
      return T[vi];
    };
#pragma unroll 3
    for (int j = t128; j < 1052; j += 128) {
      const int l1 = 2080 - 2 * j;
      const float k0 = kval(l1), k1 = kval(l1 - 1), k2 = kval(l1 - 2);
      c0[j] = pk2(k0, k1); c1[j] = pk2(k1, k2);
    }
  }
}

DEVI void ssm_conv(const Args& a, char* smem, int b, int w4, int lane, u64 tag) {
  char* ws = a.ws;
  const int ch = w4 >> 1, hf = w4 & 1, d = 2 * b + ch, t128 = hf * 64 + lane;
  wait_flags(ws, 0, blk_of(lane & 31, b >> 5), lane < 32, tag);
  STAMP(12);
  {
    const __amdgpu_buffer_rsrc_t xr = mk_rsrc(ws + OFF_XNT, XNT_BYTES);
    const u32x4 u0 = __builtin_amdgcn_raw_buffer_load_b128(xr, d * L * 2 + t128 * 16, 0, SC1);
    const u32x4 u1 = __builtin_amdgcn_raw_buffer_load_b128(xr, d * L * 2 + 2048 + t128 * 16, 0, SC1);
    char* ub = smem + SS_U + ch * SS_UP;
    *(u32x4*)(ub + 544 + t128 * 16) = u0; *(u32x4*)(ub + 544 + 2048 + t128 * 16) = u1;
  }
  gsync(smem, 2 + ch, 6);
  const int aa = lane & 15, q = lane >> 4;
  f32x4 acc[8];
#pragma unroll
  for (int e = 0; e < 8; e++) acc[e] = (f32x4){0.f, 0.f, 0.f, 0.f};
  {
    const char* kb = smem + SS_KR + (2 * ch + (aa & 1)) * SS_KP + (2088 - aa - 8 * q - (aa & 1)) * 2;
    const char* ub = smem + SS_U + ch * SS_UP + 560 + 32 * aa - 16 * q;
    auto afrag = [&](int s) -> u32x4 { const unsigned* p = (const unsigned*)(kb - 64 * s); return (u32x4){p[0], p[1], p[2], p[3]}; };
    auto bfrag = [&](int sg) -> u32x4 { return *(const u32x4*)(ub - 64 * sg); };
    if (hf == 0) {
      const u32x4 b8 = bfrag(8);
#pragma unroll
      for (int cc = 0; cc < 8; cc++) acc[cc] = mfma16(afrag(8 * (cc + 1)), b8, acc[cc]);
    }
    u32x4 af[2][8], bfr[2][8];
#pragma unroll
    for (int p = 0; p < 8; p++) { af[0][p] = afrag(8 * p + 4 * hf); bfr[0][p] = bfrag(4 * hf - 8 * p); }
#pragma unroll
    for (int ii = 0; ii < 4; ii++) {
      if (ii < 3) {
#pragma unroll
        for (int p = 0; p < 8; p++) { af[(ii + 1) & 1][p] = afrag(8 * p + 4 * hf + ii + 1); bfr[(ii + 1) & 1][p] = bfrag(4 * hf + ii + 1 - 8 * p); }
      }
#pragma unroll
      for (int cc = 0; cc < 8; cc++)
#pragma unroll
        for (int p = 0; p <= cc; p++) acc[cc] = mfma16(af[ii & 1][p], bfr[ii & 1][cc - p], acc[cc]);
    }
  }
  STAMP(13);
  gsync(smem, 2 + ch, 8);
  char* red = smem + SS_KR + (2 * ch) * SS_KP;
  if (hf == 0) {
#pragma unroll
    for (int e = 0; e < 4; e++) *(f32x4*)(red + e * 1024 + lane * 16) = acc[4 + e];
  } else {
#pragma unroll
    for (int e = 0; e < 4; e++) *(f32x4*)(red + 4096 + e * 1024 + lane * 16) = acc[e];
  }
  gsync(smem, 2 + ch, 10);
  const float dsk = bf2f(a.Dskip[d]);
  const __amdgpu_buffer_rsrc_t yr = mk_rsrc(ws + OFF_YAT, XNT_BYTES);
#pragma unroll
  for (int e = 0; e < 4; e++) {
    const f32x4 o4 = *(const f32x4*)(red + (hf ? 0 : 4096) + e * 1024 + lane * 16);
    const f32x4 alo = acc[e], ahi = acc[4 + e];
    const f32x4 mine = hf ? ahi : alo;
    const int t = 256 * (4 * hf + e) + 16 * aa + 4 * q;
    const u32x2 uv = *(const u32x2*)(smem + SS_U + ch * SS_UP + 544 + t * 2);
    const float y0 = rbf(mine[0] + o4[0]) + rbf(lo2f(uv[0]) * dsk), y1 = rbf(mine[1] + o4[1]) + rbf(hi2f(uv[0]) * dsk);
    const float y2 = rbf(mine[2] + o4[2]) + rbf(lo2f(uv[1]) * dsk), y3 = rbf(mine[3] + o4[3]) + rbf(hi2f(uv[1]) * dsk);
    __builtin_amdgcn_raw_buffer_store_b64((u32x2){pk2(y0, y1), pk2(y2, y3)}, yr, (d * L + t) * 2, 0, SC1);
  }
  WAIT_VM(0);
  gsync(smem, 4, 4);
  if (w4 == 0 && lane == 0) set_flag(ws, 2, b, tag);
  STAMP(14);
}

DEVI void phase3(const Args& a, char* smem, int rt, int dt, int w, int lane, u64 tag) {
  char* ws = a.ws;
  const int c = lane & 15, q = lane >> 4;
  char* wsl = smem + w * 28672;
  {
    const int row = lane >> 3, slot = lane & 7;
    const int chunk = (((slot >> 1) ^ ((row >> 1) & 3)) << 1) | (slot & 1);
    const u16* src = a.w_out + (size_t)(224 * w + row) * D + 64 * dt + chunk * 8;
#pragma unroll 4
    for (int k = 0; k < 28; k++) glds16(src + (size_t)(8 * k) * D, wsl + k * 1024);
  }
  const int t = 64 * rt + trow(w, c);
  u32x2 xv[4];
#pragma unroll
  for (int n = 0; n < 4; n++) xv[n] = *(const u32x2*)(a.x + (size_t)t * D + 64 * dt + 16 * n + 4 * q);
  wait_flags(ws, 2, 32 * dt + (lane & 31), lane < 32, tag);
  {
    const __amdgpu_buffer_rsrc_t yr = mk_rsrc(ws + OFF_YAT, XNT_BYTES);
#pragma unroll
    for (int e = 0; e < 2; e++) {
      const int row = 16 * w + 8 * e + (lane >> 3);
      const u32x4 yv = __builtin_amdgcn_raw_buffer_load_b128(yr, ((64 * dt + row) * L + 64 * rt) * 2 + (lane & 7) * 16, 0, SC1);
      *(u32x4*)(smem + LDS_MISC + row * 128 + (lane & 7) * 16) = yv;
    }
  }
  wait_flags(ws, 1, blk_of(rt, 2 * w + (lane & 1)), lane < 2, tag);
  STAMP(16);
  u32x4 hf[7][4];
  {
    const unsigned voff = lane * 16;
#pragma unroll
    for (int i = 0; i < 4; i++) {
      const char* hb = ws + OFF_HF + (size_t)((4 * rt + ((w + i) & 3)) * 28 + 7 * w) * 1024 + 3072;
      asm volatile("global_load_dwordx4 %0, %7, %8 offset:-3072 sc1\n\tglobal_load_dwordx4 %1, %7, %8 offset:-2048 sc1\n\t"
                   "global_load_dwordx4 %2, %7, %8 offset:-1024 sc1\n\tglobal_load_dwordx4 %3, %7, %8 sc1\n\t"
                   "global_load_dwordx4 %4, %7, %8 offset:1024 sc1\n\tglobal_load_dwordx4 %5, %7, %8 offset:2048 sc1\n\t"
                   "global_load_dwordx4 %6, %7, %8 offset:3072 sc1"
                   : "=&v"(hf[0][i]), "=&v"(hf[1][i]), "=&v"(hf[2][i]), "=&v"(hf[3][i]), "=&v"(hf[4][i]), "=&v"(hf[5][i]), "=&v"(hf[6][i])
                   : "v"(voff), "s"(hb) : "memory");
    }
#pragma unroll
    for (int s = 0; s < 7; s++) asm volatile("s_waitcnt vmcnt(0)" : "+v"(hf[s][0]), "+v"(hf[s][1]), "+v"(hf[s][2]), "+v"(hf[s][3]) :: "memory");
  }
  f32x4 acc[4][4];
#pragma unroll
  for (int i = 0; i < 4; i++)
#pragma unroll
    for (int n = 0; n < 4; n++) acc[i][n] = (f32x4){0.f, 0.f, 0.f, 0.f};
  {
    const int qq = (lane & 15) >> 2, p = lane & 3, g = ((q & 1) << 1) | (qq >> 1);
    const char* wb = wsl + (4 * q + qq) * 128 + 8 * p;
#pragma unroll
    for (int s = 0; s < 7; s++) {
      u32x4 wf[4];
#pragma unroll
      for (int n = 0; n < 4; n++) { const char* pp = wb + s * 4096 + ((n ^ g) * 32); wf[n] = ldtr(pp, pp + 2048); }
#pragma unroll
      for (int i = 0; i < 4; i++)
#pragma unroll
        for (int n = 0; n < 4; n++) acc[i][n] = mfma16(wf[n], hf[s][i], acc[i][n]);
    }
  }
  WAIT_LGKM0();
#pragma unroll
  for (int i = 1; i < 4; i++)
#pragma unroll
    for (int n = 0; n < 4; n++) *(f32x4*)(wsl + ((i - 1) * 4 + n) * 1024 + lane * 16) = acc[i][n];
  gsync(smem, 0, 12);
#pragma unroll
  for (int i = 1; i < 4; i++)
#pragma unroll
    for (int n = 0; n < 4; n++) acc[0][n] += *(const f32x4*)(smem + ((w - i) & 3) * 28672 + ((i - 1) * 4 + n) * 1024 + lane * 16);
  {
    const int qq = (lane & 15) >> 2, p = lane & 3;
#pragma unroll
    for (int n = 0; n < 4; n++) {
      const char* yp = smem + LDS_MISC + (16 * n + 4 * q + qq) * 128 + 64 * (w >> 1) + 16 * p + 8 * (w & 1);
      const u32x2 ya = __builtin_bit_cast(u32x2, __builtin_amdgcn_ds_read_tr16_b64_v4i16((LAS s16x4*)yp));
      const float o0 = rbf(lo2f(xv[n][0]) + lo2f(ya[0])) + rbf(acc[0][n][0]), o1 = rbf(hi2f(xv[n][0]) + hi2f(ya[0])) + rbf(acc[0][n][1]);
      const float o2 = rbf(lo2f(xv[n][1]) + lo2f(ya[1])) + rbf(acc[0][n][2]), o3 = rbf(hi2f(xv[n][1]) + hi2f(ya[1])) + rbf(acc[0][n][3]);
      *(u32x2*)(a.out + (size_t)t * D + 64 * dt + 16 * n + 4 * q) = (u32x2){pk2(o0, o1), pk2(o2, o3)};
    }
  }
  STAMP(17);
}

#define SPLIT 0
__global__ __launch_bounds__(512, 2) void fused_kernel(Args a, int phases) {
  extern __shared__ __attribute__((aligned(16))) char smem[];
  char* ws = a.ws;
  const int b = blockIdx.x, xcd = b & 7, i0 = b >> 3, rt = 8 * (xcd >> 1) + (i0 & 7), ct = 4 * (xcd & 1) + (i0 >> 3);
  const int lane = threadIdx.x & 63, w = __builtin_amdgcn_readfirstlane(threadIdx.x >> 6);
  STAMP(0);
  const u64 tag = *(const u64*)(ws + OFF_NONCE) + 1;
  if (threadIdx.x < 8) ((unsigned*)(smem + LDS_SYNC))[threadIdx.x] = 0u;
  if (!SPLIT || (phases & 1)) phase1(a, smem, rt, ct, w, lane, tag);
  if (w < 4) {
    if (!SPLIT || (phases & 2)) {
      p2_wstage(a, smem, rt, w, lane, 0, 0); p2_wstage(a, smem, rt, w, lane, 1, 1); p2_wstage(a, smem, rt, w, lane, 2, 2);
      phase2_gemm(a, smem, rt, ct, w, lane, tag);
    }
    if (!SPLIT || (phases & 4)) {
      if (SPLIT && !(phases & 2)) { if (lane == 0) atomicAdd((unsigned*)(smem + LDS_SYNC), 2u); }
      phase3(a, smem, rt, ct, w, lane, tag);
    }
  } else {
    if (!SPLIT || (phases & 2)) {
      ssm_kgen(a, smem, b, w - 4, lane);
      STAMP(11);
      ssm_conv(a, smem, b, w - 4, lane, tag);
    }
    if (b == 0 && w == 4 && (!SPLIT || (phases & 4))) {
#pragma unroll 1
      for (int k = 0; k < 4; k++) wait_flags(ws, 0, k * 64 + lane, true, tag);
      if (lane == 0) __hip_atomic_store((gu64*)(ws + OFF_NONCE), tag, RLX_AGENT);
    }
  }
}

extern "C" void kernel_launch(void* const* d_in, const int* in_sizes, int n_in, void* d_out, int out_size, void* d_ws, size_t ws_size,
                              hipStream_t stream) {
  Args a;
  a.x = (const u16*)d_in[0];
  a.Are = (const u16*)d_in[1]; a.Aim = (const u16*)d_in[2]; a.Bre = (const u16*)d_in[3]; a.Bim = (const u16*)d_in[4];
  a.Cre = (const u16*)d_in[5]; a.Cim = (const u16*)d_in[6]; a.logdt = (const u16*)d_in[7]; a.Dskip = (const u16*)d_in[8];
  a.w_in = (const u16*)d_in[10]; a.w_sp = (const u16*)d_in[11]; a.b_sp = (const u16*)d_in[12]; a.w_out = (const u16*)d_in[13];
  a.out = (u16*)d_out; a.ws = (char*)d_ws;
  (void)hipFuncSetAttribute((const void*)fused_kernel, hipFuncAttributeMaxDynamicSharedMemorySize, LDS_TOTAL);
  fused_kernel<<<256, 512, LDS_TOTAL, stream>>>(a, 7);
}
```

```cpp
#include <hip/hip_runtime.h>
#include <hip/hip_bf16.h>
#include <stdint.h>

typedef unsigned short u16;
typedef unsigned long long u64;
typedef __bf16 bf16x2 __attribute__((ext_vector_type(2)));
typedef __bf16 bf16x8 __attribute__((ext_vector_type(8)));
typedef short s16x4 __attribute__((ext_vector_type(4)));
typedef float f32x2 __attribute__((ext_vector_type(2)));
typedef float f32x4 __attribute__((ext_vector_type(4)));
typedef float f32x16 __attribute__((ext_vector_type(16)));
typedef unsigned u32x2 __attribute__((ext_vector_type(2)));
typedef unsigned u32x4 __attribute__((ext_vector_type(4)));
#define LAS __attribute__((address_space(3)))
#define GAS __attribute__((address_space(1)))
#define DEVI __device__ __forceinline__
#define RLX_AGENT __ATOMIC_RELAXED, __HIP_MEMORY_SCOPE_AGENT

constexpr int L = 2048, D = 512, F = 1792, FH = 896, NS = 128;
constexpr size_t OFF_Z2F = 0;
constexpr size_t OFF_XNT = OFF_Z2F + (size_t)8 * 64 * 7 * 1024;
constexpr size_t OFF_HF = OFF_XNT + (size_t)512 * 2048 * 2;
constexpr size_t OFF_YAT = OFF_HF + (size_t)128 * 28 * 1024;
constexpr size_t OFF_EX = OFF_YAT + (size_t)512 * 2048 * 2;
constexpr size_t OFF_FLG = OFF_EX + (size_t)32 * 8 * 64 * 16;
constexpr size_t OFF_NONCE = OFF_FLG + 3 * 256 * 128;
constexpr size_t OFF_Z1DBG = OFF_NONCE + 4096;
constexpr unsigned Z2F_BYTES = 8 * 64 * 7 * 1024, XNT_BYTES = 512 * 2048 * 2, HF_BYTES = 128 * 28 * 1024, EX_BYTES = 32 * 8 * 64 * 16;

constexpr int P1_STAGE = 40960;
constexpr int LDS_Z1 = 98304;
constexpr int LDS_SSM = 114688;
constexpr int LDS_MISC = 151552;
constexpr int LDS_SYNC = 159744;
constexpr int LDS_TOTAL = 160768;
constexpr int SS_P = LDS_SSM, SS_T = SS_P + 4096, SS_TP = 2592  , SS_KR = SS_T + 4 * SS_TP, SS_KP = 4288;
constexpr int SS_U = SS_P, SS_UP = 4672;
static_assert(2 * SS_UP <= 4096 + 4 * SS_TP, "alias");
static_assert(SS_KR + 4 * SS_KP <= LDS_MISC, "ssm lds");
static_assert((SS_KR % 16) == 0 && (SS_T % 16) == 0, "align");

#define STAMP(i)
#define CLK() 0ull
#define PUTV(i,v)

DEVI unsigned pk2(float a, float b) { f32x2 v = {a, b}; bf16x2 r = __builtin_convertvector(v, bf16x2); return __builtin_bit_cast(unsigned, r); }
DEVI float asf(unsigned u) { return __builtin_bit_cast(float, u); }
DEVI unsigned asu(float f) { return __builtin_bit_cast(unsigned, f); }
DEVI float lo2f(unsigned u) { return __builtin_bit_cast(float, u << 16); }
DEVI float hi2f(unsigned u) { return __builtin_bit_cast(float, u & 0xffff0000u); }
DEVI float rbf(float a) { return hi2f(pk2(a, a)); }
DEVI float bf2f(u16 v) { return __builtin_bit_cast(float, ((unsigned)v) << 16); }
DEVI float dot2(unsigned a, unsigned b, float c) { return __builtin_amdgcn_fdot2_f32_bf16(__builtin_bit_cast(bf16x2, a), __builtin_bit_cast(bf16x2, b), c, false); }
DEVI f32x4 mfma16(u32x4 a, u32x4 b, f32x4 c) { return __builtin_amdgcn_mfma_f32_16x16x32_bf16(__builtin_bit_cast(bf16x8, a), __builtin_bit_cast(bf16x8, b), c, 0, 0, 0); }
DEVI f32x16 mfma32(u32x4 a, u32x4 b, f32x16 c) { return __builtin_amdgcn_mfma_f32_32x32x16_bf16(__builtin_bit_cast(bf16x8, a), __builtin_bit_cast(bf16x8, b), c, 0, 0, 0); }
DEVI float gelu_tanh(float x) {
  const float e = __builtin_amdgcn_exp2f(x * (-2.3022082f + -0.10294324f * x * x));
  return x * __builtin_amdgcn_rcpf(1.0f + e);
}
template <int CTRL> DEVI float dpp_mov(float v) { return __builtin_bit_cast(float, __builtin_amdgcn_update_dpp(0, __builtin_bit_cast(int, v), CTRL, 0xf, 0xf, false)); }
DEVI float row16_sum(float v) { v += dpp_mov<0x128>(v); v += dpp_mov<0x124>(v); v += dpp_mov<0x122>(v); v += dpp_mov<0x121>(v); return v; }
DEVI float quad4_sum(float v) {
  auto a = __builtin_amdgcn_permlane32_swap(asu(v), asu(v), false, false); v = asf((unsigned)a[0]) + asf((unsigned)a[1]);
  auto b = __builtin_amdgcn_permlane16_swap(asu(v), asu(v), false, false); return asf((unsigned)b[0]) + asf((unsigned)b[1]);
}
DEVI void ln_stats(float s, float ss, float inv_n, float& mb, float& sdb) {
  const float m = s * inv_n; const float v = fmaxf(ss * inv_n - m * m, 0.0f);
  mb = rbf(m); sdb = rbf(__builtin_amdgcn_sqrtf(rbf(rbf(v) + 1e-5f)));
}
#define WAIT_VM(n) asm volatile("s_waitcnt vmcnt(" #n ")" ::: "memory")
#define WAIT_LGKM0() asm volatile("s_waitcnt lgkmcnt(0)" ::: "memory")
#define BAR() __builtin_amdgcn_s_barrier()
#define LBAR() do { WAIT_LGKM0(); BAR(); } while (0)
DEVI void glds16u(const void* ubase, unsigned voff, char* lds) {
  unsigned keep;
  const unsigned ldsa = (unsigned)(size_t)(LAS char*)lds;
  asm volatile("s_mov_b32 %0, m0\n\ts_mov_b32 m0, %3\n\ts_nop 0\n\tglobal_load_lds_dwordx4 %1, %2\n\ts_mov_b32 m0, %0"
               : "=&s"(keep) : "v"(voff), "s"(ubase), "s"(ldsa));
}
#define SB() __builtin_amdgcn_sched_barrier(0)
DEVI u32x4 ldtr(const char* p0, const char* p1) {
  const u32x2 a = __builtin_bit_cast(u32x2, __builtin_amdgcn_ds_read_tr16_b64_v4i16((LAS s16x4*)p0));
  const u32x2 b = __builtin_bit_cast(u32x2, __builtin_amdgcn_ds_read_tr16_b64_v4i16((LAS s16x4*)p1));
  return (u32x4){a[0], a[1], b[0], b[1]};
}
DEVI int trow(int Tt, int c) { return 32 * (Tt >> 1) + 8 * (c >> 2) + (c & 3) + 4 * (Tt & 1); }
constexpr unsigned SPIN_CAP = 1u << 22;
DEVI void gsync(char* smem, int idx, unsigned target) {
  volatile LAS unsigned* cnt = (volatile LAS unsigned*)(smem + LDS_SYNC) + idx;
  WAIT_LGKM0();
  if ((threadIdx.x & 63) == 0) __hip_atomic_fetch_add((LAS unsigned*)cnt, 1u, __ATOMIC_RELAXED, __HIP_MEMORY_SCOPE_WORKGROUP);
  for (unsigned s = 0; s < SPIN_CAP; s++) { if ((unsigned)__builtin_amdgcn_readfirstlane(*cnt) >= target) break; __builtin_amdgcn_s_sleep(0); }
  asm volatile("" ::: "memory");
}
typedef GAS u64 gu64;
DEVI void wait_flags(char* ws, int which, int blk, bool active, u64 tag) {
  gu64* p = (gu64*)(ws + OFF_FLG + ((size_t)which * 256 + blk) * 128);
  for (unsigned s = 0; s < SPIN_CAP; s++) {
    const u64 v = active ? __hip_atomic_load(p, RLX_AGENT) : tag;
    if (__all(v == tag)) break;
    __builtin_amdgcn_s_sleep(1);
  }
  asm volatile("" ::: "memory");
}
DEVI void set_flag(char* ws, int which, int blk, u64 tag) { __hip_atomic_store((gu64*)(ws + OFF_FLG + ((size_t)which * 256 + blk) * 128), tag, RLX_AGENT); }
DEVI __amdgpu_buffer_rsrc_t mk_rsrc(const void* p, unsigned bytes) { return __builtin_amdgcn_make_buffer_rsrc((void*)p, 0, bytes, 0x00020000); }
constexpr int SC1 = 16;

struct Args {
  const u16 *x, *Are, *Aim, *Bre, *Bim, *Cre, *Cim, *logdt, *Dskip, *w_in, *w_sp, *b_sp, *w_out;
  u16* out; char* ws;
};

struct P1Frag { u32x4 xf[2], w1[2], w2[2]; };
DEVI void p2_wpiece(const Args& a, char* smem, int rt, int w, int lane, int j, int slot, int pc);

template <bool W3>
DEVI void p1_loop(const Args& a, char* smem, int rt, int ct, int w, int lane, int nt0, int nt1, f32x4 (&acc1)[2][2], f32x4 (&acc2)[2][2], float& cw1, float& cw2) {
  const int wm = w & 1;
  const int r = lane & 15, q = lane >> 4;
  const unsigned xvo = (unsigned)((8 * w + (lane >> 3)) * D + (((lane & 7) ^ (((lane >> 3) & 3) | ((w & 1) << 2))) * 8)) * 2u;
  const int hi = lane >> 5, s16 = lane & 31;
  const int pe = min(((s16 >> 1) & 7) ^ (hi | ((w & 1) << 2)), 6), po = min(((s16 >> 1) & 7) ^ ((2 + hi) | ((w & 1) << 2)), 6);
  const unsigned wve = (unsigned)((8 * w + hi) * F + (s16 >> 4) * FH + pe * 16 + (s16 & 1) * 8) * 2u;
  const unsigned wvo = (unsigned)((8 * w + 2 + hi) * F + (s16 >> 4) * FH + po * 16 + (s16 & 1) * 8) * 2u;
  const u16* xub = a.x + (size_t)(64 * rt) * D;
  const u16* wub = a.w_in + 112 * ct;
  const int rot = (rt + 2 * ct) & 7;
  auto stage1 = [&](int j, int slot, int pc) {
    char* sb = smem + slot * P1_STAGE;
    const int k0 = 64 * ((j + rot) & 7);
    if (pc == 0) glds16u(xub + k0, xvo, sb + w * 1024);
    else glds16u(wub + (size_t)(k0 + (pc >= 3 ? 4 : 0)) * F, (pc & 1) ? wve : wvo, sb + 8192 + w * 4096 + (((pc - 1) & 1) + (pc >= 3 ? 2 : 0)) * 1024);
  };
  auto stage = [&](int j, int slot) {
#pragma unroll
    for (int pc = 0; pc < 5; pc++) stage1(j, slot, pc);
  };
  stage(0, 0); stage(1, 1); stage(2, 2);
  const int sx = (r & 3) | (((r >> 2) & 1) << 2);
  const int xrow = 32 * wm + 8 * (r >> 2) + (r & 3);
  const int xo0 = xrow * 128 + ((q ^ sx) * 16), xo1 = xrow * 128 + (((4 + q) ^ sx) * 16);
  const int qq = (lane & 15) >> 2, p = lane & 3, swz = qq | ((q & 1) << 2);
  const int wo0 = 8192 + (8 * q + qq) * 512 + ((nt0 ^ swz) * 32) + 8 * p;
  const int wo1 = 8192 + (8 * q + qq) * 512 + ((nt1 ^ swz) * 32) + 8 * p;
  auto readf = [&](P1Frag& f, const char* sb, int kk) {
    const int xo = kk ? xo1 : xo0;
    const char* b0 = sb + wo0 + kk * 16384; const char* b1 = sb + wo1 + kk * 16384;
    f.xf[0] = *(const u32x4*)(sb + xo);
    f.w2[0] = ldtr(b0 + 256, b0 + 2304); f.w1[0] = ldtr(b0, b0 + 2048);
    if (!W3) { f.w2[1] = ldtr(b1 + 256, b1 + 2304); f.w1[1] = ldtr(b1, b1 + 2048); }
    f.xf[1] = *(const u32x4*)(sb + xo + 512);
  };
  const unsigned ONES = 0x3f803f80u;
  const u32x4 ONES4 = {ONES, ONES, ONES, ONES};
  float c1[4] = {0.f, 0.f, 0.f, 0.f}, c2[4] = {0.f, 0.f, 0.f, 0.f};
  auto half = [&](const P1Frag& f) {
    acc2[0][0] = mfma16(f.w2[0], f.xf[0], acc2[0][0]); acc1[0][0] = mfma16(f.w1[0], f.xf[0], acc1[0][0]);
    if (W3) { acc1[0][1] = mfma16(ONES4, f.xf[0], acc1[0][1]); acc2[0][1] = mfma16(f.xf[0], f.xf[0], acc2[0][1]); }
    else { acc2[0][1] = mfma16(f.w2[1], f.xf[0], acc2[0][1]); acc1[0][1] = mfma16(f.w1[1], f.xf[0], acc1[0][1]); }
    acc2[1][0] = mfma16(f.w2[0], f.xf[1], acc2[1][0]); acc1[1][0] = mfma16(f.w1[0], f.xf[1], acc1[1][0]);
    if (W3) { acc1[1][1] = mfma16(ONES4, f.xf[1], acc1[1][1]); acc2[1][1] = mfma16(f.xf[1], f.xf[1], acc2[1][1]); }
    else { acc2[1][1] = mfma16(f.w2[1], f.xf[1], acc2[1][1]); acc1[1][1] = mfma16(f.w1[1], f.xf[1], acc1[1][1]); }
#pragma unroll
    for (int e = 0; e < 4; e++) { c1[e] = dot2(f.w1[0][e], ONES, c1[e]); c2[e] = dot2(f.w2[0][e], ONES, c2[e]); }
  };
#define SGB(mask, n) __builtin_amdgcn_sched_group_barrier(mask, n, 0)
#define P1_PIPE() do { _Pragma("unroll") for (int k_ = 0; k_ < 5; k_++) { SGB(0x008, 1); SGB(0x100, 2); SGB(0x002, 1); } _Pragma("unroll") for (int k_ = 0; k_ < 3; k_++) { SGB(0x008, 1); SGB(0x002, 1); } } while (0)
  P1Frag fa, fb;
  WAIT_VM(10); BAR();
  readf(fa, smem, 0);
  int slot = 0;
#pragma unroll 1
  for (int j = 0; j < 7; j++) {
    const char* sb = smem + slot * P1_STAGE;
    const int nslot = slot == 2 ? 0 : slot + 1;
    SB();
    readf(fb, sb, 1);
    half(fa);
    P1_PIPE();
    SB();
    if (j < 6) WAIT_VM(5); else WAIT_VM(0);
    LBAR();
    if (j <= 4) {
      SB();
      stage1(j + 3, slot, 0);
      readf(fa, smem + nslot * P1_STAGE, 0);
      half(fb);
      P1_PIPE();
      SB();
      stage1(j + 3, slot, 1); stage1(j + 3, slot, 2); stage1(j + 3, slot, 3); stage1(j + 3, slot, 4);
      SB();
    } else {
      SB();
      readf(fa, smem + nslot * P1_STAGE, 0);
      half(fb);
      P1_PIPE();
      SB();
    }
    slot = nslot;
  }
  readf(fb, smem + slot * P1_STAGE, 1);
  half(fa);
  P1_PIPE();
  SB();
  half(fb);
  cw1 = quad4_sum((c1[0] + c1[1]) + (c1[2] + c1[3]));
  cw2 = quad4_sum((c2[0] + c2[1]) + (c2[2] + c2[3]));
}

DEVI void phase1(const Args& a, char* smem, int rt, int ct, int w, int lane, u64 tag) {
  char* ws = a.ws;
  const int wm = w & 1, wn = w >> 1;
  const int r = lane & 15, q = lane >> 4;
  const int xrow = 32 * wm + 8 * (r >> 2) + (r & 3);
  const int qq = (lane & 15) >> 2, p = lane & 3;
  const int nt0 = wn == 3 ? 6 : 2 * wn + wm, nt1 = wn == 3 ? 7 : 2 * wn + (wm ^ 1);
  f32x4 acc1[2][2], acc2[2][2];
#pragma unroll
  for (int m = 0; m < 2; m++)
#pragma unroll
    for (int i = 0; i < 2; i++) { acc1[m][i] = (f32x4){0.f, 0.f, 0.f, 0.f}; acc2[m][i] = (f32x4){0.f, 0.f, 0.f, 0.f}; }
  float cw1, cw2;
  if (wn == 3) p1_loop<true>(a, smem, rt, ct, w, lane, nt0, nt1, acc1, acc2, cw1, cw2);
  else p1_loop<false>(a, smem, rt, ct, w, lane, nt0, nt1, acc1, acc2, cw1, cw2);
  STAMP(2);
  f32x4* rstat = (f32x4*)(smem + LDS_MISC + 4096);
  float* cwt = (float*)(smem + LDS_MISC + 5120);
  f32x2* part = (f32x2*)(smem + LDS_MISC + 6144);
  f32x2* st2 = (f32x2*)(smem + LDS_MISC + 2048);
  if (q == 0) { cwt[nt0 * 16 + r] = cw1; cwt[(8 + nt0) * 16 + r] = cw2; }
  if (wn == 3 && q == (r >> 2)) {
#pragma unroll
    for (int m = 0; m < 2; m++) {
      const f32x4 g = acc2[m][1];
      const float ss = (r & 2) ? ((r & 1) ? g[3] : g[2]) : ((r & 1) ? g[1] : g[0]);
      float mb, sdb; ln_stats(acc1[m][1][0], ss, 1.0f / D, mb, sdb);
      const float ri = __builtin_amdgcn_rcpf(sdb);
      rstat[xrow + 4 * m] = (f32x4){ri, ri * mb, mb, sdb};
    }
  }
  const int tid = w * 64 + lane;
  const u32x4 xv = *(const u32x4*)(a.x + (size_t)(64 * rt + (tid >> 3)) * D + 64 * ct + (tid & 7) * 8);
  LBAR();
  const int prot = 0;
#define P1_WPF(k) do { const int id = (w >> 2) * 12 + (k); p2_wpiece(a, smem, rt, w & 3, lane, ((id >> 3) + prot) & 7, id >> 3, id & 7); } while (0)
  float v2[2][2][4];
  f32x4 rs[2];
#pragma unroll
  for (int m = 0; m < 2; m++) rs[m] = rstat[xrow + 4 * m];
  {
    float s1[2] = {0.f, 0.f}, s2[2] = {0.f, 0.f};
#pragma unroll
    for (int i = 0; i < 2; i++) {
      const f32x4 cw = *(const f32x4*)(cwt + (8 + (i ? nt1 : nt0)) * 16 + 4 * q);
#pragma unroll
      for (int m = 0; m < 2; m++) {
#pragma unroll
        for (int e = 0; e < 4; e++) {
          float g = gelu_tanh(rs[m][0] * acc2[m][i][e] - rs[m][1] * cw[e]);
          if (i == 1 && wn == 3) g = 0.f;
          v2[m][i][e] = g; s1[m] += g; s2[m] += g * g;
        }
        SB(); P1_WPF(2 * i + m); SB();
      }
    }
#pragma unroll
    for (int m = 0; m < 2; m++) { s1[m] = quad4_sum(s1[m]); s2[m] = quad4_sum(s2[m]); }
    if (q == 0) {
#pragma unroll
      for (int m = 0; m < 2; m++) part[wn * 64 + xrow + 4 * m] = (f32x2){s1[m], s2[m]};
    }
  }
  LBAR();
  const __amdgpu_buffer_rsrc_t exr = mk_rsrc(ws + OFF_EX, EX_BYTES);
  if (w == 7) {
    const f32x2 p0 = part[lane], p1 = part[64 + lane], p2 = part[128 + lane], p3 = part[192 + lane];
    const u32x4 gv = {asu((p0[0] + p1[0]) + (p2[0] + p3[0])), asu((p0[1] + p1[1]) + (p2[1] + p3[1])), (unsigned)tag, (unsigned)(tag >> 32)};
    __builtin_amdgcn_raw_buffer_store_b128(gv, exr, ((rt * 8 + ct) * 64 + lane) * 16, 0, SC1);
  }
  STAMP(3);
#pragma unroll
  for (int i = 0; i < 2; i++) {
    const int n = i ? nt1 : nt0;
    const f32x4 cw = *(const f32x4*)(cwt + n * 16 + 4 * q);
#pragma unroll
    for (int m = 0; m < 2; m++) {
      float v[4];
#pragma unroll
      for (int e = 0; e < 4; e++) v[e] = gelu_tanh(rs[m][0] * acc1[m][i][e] - rs[m][1] * cw[e]);
      if (n < 7) *(u32x2*)(smem + LDS_Z1 + (((2 * wm + m) * 7 + n) * 64 + lane) * 8) = (u32x2){pk2(v[0], v[1]), pk2(v[2], v[3])};
      SB(); P1_WPF(4 + 4 * i + 2 * m); P1_WPF(5 + 4 * i + 2 * m); SB();
    }
  }
  {
    const int row = tid >> 3, ch8 = (tid & 7) * 8;
    const f32x4 rr = rstat[row];
    u16* tl = (u16*)(smem + LDS_SSM);
#pragma unroll
    for (int e = 0; e < 4; e++) {
      const unsigned pk = pk2(rbf(lo2f(xv[e]) - rr[2]) * rr[0], rbf(hi2f(xv[e]) - rr[2]) * rr[0]);
      tl[(ch8 + 2 * e) * 72 + row] = (u16)pk; tl[(ch8 + 2 * e + 1) * 72 + row] = (u16)(pk >> 16);
    }
    LBAR();
    const int ch = tid >> 3, pc = tid & 7;
    const u32x4 o = *(const u32x4*)(tl + ch * 72 + pc * 8);
    __builtin_amdgcn_raw_buffer_store_b128(o, mk_rsrc(ws + OFF_XNT, XNT_BYTES), ((64 * ct + ch) * L + 64 * rt + pc * 8) * 2, 0, SC1);
  }
  STAMP(4);
  if (w == 7) {
    float s1 = 0.f, s2 = 0.f;
    for (unsigned s = 0; s < SPIN_CAP; s++) {
      bool ok = true; s1 = 0.f; s2 = 0.f;
#pragma unroll
      for (int c8 = 0; c8 < 8; c8++) {
        const u32x4 g = __builtin_amdgcn_raw_buffer_load_b128(exr, ((rt * 8 + c8) * 64 + lane) * 16, 0, SC1);
        ok &= (g[2] == (unsigned)tag) & (g[3] == (unsigned)(tag >> 32));
        s1 += asf(g[0]); s2 += asf(g[1]);
      }
      if (__all(ok)) break;
      __builtin_amdgcn_s_sleep(1);
      asm volatile("" ::: "memory");
    }
    float mb, sdb; ln_stats(s1, s2, 1.0f / FH, mb, sdb);
    st2[lane] = (f32x2){mb, __builtin_amdgcn_rcpf(sdb)};
  }
  LBAR();
  STAMP(5);
  {
    const __amdgpu_buffer_rsrc_t zr = mk_rsrc(ws + OFF_Z2F, Z2F_BYTES);
    char* zt = smem + LDS_SSM + 9216 + w * 2304;
    const int lrow = 8 * (r >> 2) + (r & 3);
#pragma unroll
    for (int m = 0; m < 2; m++) {
      const f32x2 st = st2[xrow + 4 * m];
#pragma unroll
      for (int i = 0; i < 2; i++) {
        float z[4];
#pragma unroll
        for (int e = 0; e < 4; e++) z[e] = (rbf(v2[m][i][e]) - st[0]) * st[1];
        *(u32x2*)(zt + (lrow + 4 * m) * 72 + i * 32 + 8 * q) = (u32x2){pk2(z[0], z[1]), pk2(z[2], z[3])};
      }
    }
    WAIT_LGKM0();
#pragma unroll
    for (int i = 0; i < 2; i++) {
      const int n = i ? nt1 : nt0;
      const char* tp = zt + (8 * q + qq) * 72 + i * 32 + 8 * p;
      const u32x4 o = ldtr(tp, tp + 4 * 72);
      if (n < 7) __builtin_amdgcn_raw_buffer_store_b128(o, zr, (((ct * 64 + 2 * rt + wm) * 7 + n) * 64 + lane) * 16, 0, SC1);
    }
  }
  WAIT_VM(0);
  LBAR();
  if (w == 0 && lane == 0) set_flag(ws, 0, blockIdx.x, tag);
  STAMP(6);
}

DEVI int blk_of(int rt, int ct) { return (((rt & 7) + 8 * (ct & 3)) << 3) | (((rt >> 3) << 1) | (ct >> 2)); }

#define ZLOAD1(dst, base, off) asm volatile("global_load_dwordx4 %0, %1, %2 offset:%3 sc1" : "=&v"(dst) : "v"(voff), "s"(base), "i"(off) : "memory")
#define ZWAIT(n, buf)                                                                                                      \
  asm volatile("s_waitcnt vmcnt(" #n ")" : "+v"(buf[0]), "+v"(buf[1]), "+v"(buf[2]), "+v"(buf[3]), "+v"(buf[4]), "+v"(buf[5]), "+v"(buf[6]) :: "memory")

DEVI void p2_wpiece(const Args& a, char* smem, int rt, int w, int lane, int j, int slot, int pc) {
  const unsigned vo = (unsigned)((lane >> 3) * L + ((lane & 7) ^ (((lane >> 3) & 3) | ((pc & 1) << 2))) * 8) * 2u;
  glds16u(a.w_sp + (size_t)(64 * rt + 8 * pc) * L + 256 * j + 64 * w, vo, smem + w * 24576 + slot * 8192 + pc * 1024);
}
DEVI void p2_wstage(const Args& a, char* smem, int rt, int w, int lane, int j, int slot) {
#pragma unroll
  for (int pc = 0; pc < 8; pc++) p2_wpiece(a, smem, rt, w, lane, j, slot, pc);
}

DEVI void phase2_gemm(const Args& a, char* smem, int rt, int ct, int w, int lane, u64 tag) {
  char* ws = a.ws;
  const int c = lane & 15, q = lane >> 4;
  wait_flags(ws, 0, blk_of(4 * (lane & 7) + w, ct), lane < 8, tag);
  STAMP(8);
  const u64 ck0 = CLK();
  const unsigned voff = lane * 16;
  const int rot = 0;
  const char* zb0 = ws + OFF_Z2F + (size_t)(ct * 64 + 2 * w) * 7168 + 3072;
  const char* zb = zb0 + (size_t)rot * 8 * 7168;
  u32x4 za[7], zc[7];
  {
    const char* z1p = zb + 7168;
    ZLOAD1(za[0], zb, -3072); ZLOAD1(za[1], zb, -2048); ZLOAD1(za[2], zb, -1024); ZLOAD1(za[3], zb, 0); ZLOAD1(za[4], zb, 1024); ZLOAD1(za[5], zb, 2048); ZLOAD1(za[6], zb, 3072);
    ZLOAD1(zc[0], z1p, -3072); ZLOAD1(zc[1], z1p, -2048); ZLOAD1(zc[2], z1p, -1024); ZLOAD1(zc[3], z1p, 0); ZLOAD1(zc[4], z1p, 1024); ZLOAD1(zc[5], z1p, 2048); ZLOAD1(zc[6], z1p, 3072);
  }
  const int sx = (c & 3) | (((c >> 2) & 1) << 2);
  int wo[4][2];
#pragma unroll
  for (int i = 0; i < 4; i++) {
    const int row = trow((w + i) & 3, c);
    wo[i][0] = w * 24576 + row * 128 + ((q ^ sx) * 16); wo[i][1] = w * 24576 + row * 128 + (((4 + q) ^ sx) * 16);
  }
  f32x4 acc[4][7];
#pragma unroll
  for (int i = 0; i < 4; i++)
#pragma unroll
    for (int n = 0; n < 7; n++) acc[i][n] = (f32x4){0.f, 0.f, 0.f, 0.f};
  u32x4 wa[4], wb[4];
#pragma unroll
  for (int i = 0; i < 4; i++) wa[i] = *(const u32x4*)(smem + wo[i][0]);
  int so_cur = 0, so_nxt = 8192, so_prv = 16384;
#define P2_MM(zbuf, wf, n) do { _Pragma("unroll") for (int i = 0; i < 4; i++) acc[i][n] = mfma16(zbuf[n], wf[i], acc[i][n]); } while (0)
#define P2_WP(pc) do { if (dow) glds16u(wnext + (size_t)(8 * pc) * L, (pc & 1) ? wvo1 : wvo0, smem + w * 24576 + so_prv + pc * 1024); } while (0)
  const unsigned wvo0 = (unsigned)((lane >> 3) * L + ((lane & 7) ^ ((lane >> 3) & 3)) * 8) * 2u;
  const unsigned wvo1 = (unsigned)((lane >> 3) * L + ((lane & 7) ^ (((lane >> 3) & 3) | 4)) * 8) * 2u;
#define P2_NIT 7
#pragma unroll 1
  for (int j = 0; j < P2_NIT; j++) {
    const bool dow = (unsigned)(j - 1) < 5u;
    const u16* wnext = a.w_sp + (size_t)(64 * rt) * L + 256 * ((j + 2 + rot) & 7) + 64 * w;
    const char* zn = zb0 + (size_t)((j + 1 + rot) & 7) * 8 * 7168;
    const char* zn1 = zn + 7168;
    if (j == 6) WAIT_VM(0);
    ZWAIT(7, za);
    P2_MM(za, wa, 0); SB(); ZLOAD1(za[0], zn, -3072); P2_WP(0); SB();
    P2_MM(za, wa, 1); SB(); ZLOAD1(za[1], zn, -2048); P2_WP(1); SB();
#pragma unroll
    for (int i = 0; i < 4; i++) wb[i] = *(const u32x4*)(smem + so_cur + wo[i][1]);
    SB();
    P2_MM(za, wa, 2); SB(); ZLOAD1(za[2], zn, -1024); P2_WP(2); SB();
    P2_MM(za, wa, 3); SB(); ZLOAD1(za[3], zn, 0); P2_WP(3); SB();
    P2_MM(za, wa, 4); SB(); ZLOAD1(za[4], zn, 1024); P2_WP(4); SB();
    P2_MM(za, wa, 5); SB(); ZLOAD1(za[5], zn, 2048); P2_WP(5); SB();
    P2_MM(za, wa, 6); SB(); ZLOAD1(za[6], zn, 3072); P2_WP(6); P2_WP(7); SB();
    ZWAIT(7, zc);
    P2_MM(zc, wb, 0); SB(); ZLOAD1(zc[0], zn1, -3072); SB();
    P2_MM(zc, wb, 1); SB(); ZLOAD1(zc[1], zn1, -2048); SB();
#pragma unroll
    for (int i = 0; i < 4; i++) wa[i] = *(const u32x4*)(smem + so_nxt + wo[i][0]);
    SB();
    P2_MM(zc, wb, 2); SB(); ZLOAD1(zc[2], zn1, -1024); SB();
    P2_MM(zc, wb, 3); SB(); ZLOAD1(zc[3], zn1, 0); SB();
    P2_MM(zc, wb, 4); SB(); ZLOAD1(zc[4], zn1, 1024); SB();
    P2_MM(zc, wb, 5); SB(); ZLOAD1(zc[5], zn1, 2048); SB();
    P2_MM(zc, wb, 6); SB(); ZLOAD1(zc[6], zn1, 3072); SB();
    const int t = so_cur; so_cur = so_nxt; so_nxt = so_prv; so_prv = t;
  }
  {
    ZWAIT(7, za);
#pragma unroll
    for (int i = 0; i < 4; i++) wb[i] = *(const u32x4*)(smem + so_cur + wo[i][1]);
#pragma unroll
    for (int n = 0; n < 7; n++) P2_MM(za, wa, n);
    ZWAIT(0, zc);
#pragma unroll
    for (int n = 0; n < 7; n++) P2_MM(zc, wb, n);
  }
  PUTV(8, CLK() - ck0);
  STAMP(9);
#pragma unroll
  for (int i = 1; i < 4; i++)
#pragma unroll
    for (int n = 0; n < 7; n++) *(f32x4*)(smem + w * 24576 + ((i - 1) * 7 + n) * 1024 + lane * 16) = acc[i][n];
  const float bsp = bf2f(a.b_sp[64 * rt + trow(w, c)]);
  gsync(smem, 0, 4);
#pragma unroll
  for (int i = 1; i < 4; i++)
#pragma unroll
    for (int n = 0; n < 7; n++) acc[0][n] += *(const f32x4*)(smem + ((w - i) & 3) * 24576 + ((i - 1) * 7 + n) * 1024 + lane * 16);
  {
    const __amdgpu_buffer_rsrc_t hr = mk_rsrc(ws + OFF_HF, HF_BYTES);
    const int T = 4 * rt + w;
#pragma unroll
    for (int n = 0; n < 7; n++) {
      const u32x2 z1 = *(const u32x2*)(smem + LDS_Z1 + ((w * 7 + n) * 64 + lane) * 8);
      float g[4];
#pragma unroll
      for (int e = 0; e < 4; e++) g[e] = rbf(rbf(acc[0][n][e]) + bsp);
      const u32x2 o = {pk2(lo2f(z1[0]) * g[0], hi2f(z1[0]) * g[1]), pk2(lo2f(z1[1]) * g[2], hi2f(z1[1]) * g[3])};
      const int Ft = 7 * ct + n;
      __builtin_amdgcn_raw_buffer_store_b64(o, hr, ((T * 28 + (Ft >> 1)) * 64 + lane) * 16 + (Ft & 1) * 8, 0, SC1);
    }
  }
  WAIT_VM(0);
  gsync(smem, 0, 8);
  if (w == 0 && lane == 0) set_flag(ws, 1, blockIdx.x, tag);
  STAMP(10);
}

DEVI void ssm_kgen(const Args& a, char* smem, int b, int w4, int lane) {
  char* ws = a.ws; (void)ws;
  const int ch = w4 >> 1, hf = w4 & 1, d = 2 * b + ch, t128 = hf * 64 + lane;
  float* T = (float*)(smem + SS_T + (2 * ch + hf) * SS_TP);
  {
    const int i = d * NS + t128;
    const float dt = rbf(__expf(bf2f(a.logdt[d])));
    const float zr = dt * bf2f(a.Are[i]), zi = dt * bf2f(a.Aim[i]);
    const float br = dt * bf2f(a.Bre[i]), bi = dt * bf2f(a.Bim[i]);
    const float cr = bf2f(a.Cre[i]), ci = bf2f(a.Cim[i]);
    *(f32x4*)(smem + SS_P + (ch * 128 + t128) * 16) = (f32x4){zr * 1.4426950408889634f, zi * 0.15915494309189535f, cr * br - ci * bi, cr * bi + ci * br};
    if (lane < 8) T[640 + lane] = 0.f;
  }
  gsync(smem, 2 + ch, 2);
  STAMP(18);
  {
    const int r = lane & 31, h = lane >> 5;
    const float la = (float)(r < 8 ? r : r < 12 ? 256 + 2 * (r - 8) : r < 14 ? 512 + 4 * (r - 12) : r < 16 ? 768 + 4 * (r - 14) : r < 20 ? 1024 + 256 * (r - 16) : r == 20 ? 2048 : 0);
    const float lb = (float)(8 * r);
    f32x16 kacc;
#pragma unroll
    for (int e = 0; e < 16; e++) kacc[e] = 0.f;
#pragma unroll 2
    for (int s = 0; s < 8; s++) {
      u32x4 uf, vf;
#pragma unroll
      for (int e = 0; e < 4; e++) {
        const f32x4 p = *(const f32x4*)(smem + SS_P + (ch * 128 + 64 * hf + 8 * s + 4 * h + e) * 16);
        const float ma = __builtin_amdgcn_exp2f(p[0] * la), pa = __builtin_amdgcn_fractf(p[1] * la);
        const float ca = __builtin_amdgcn_cosf(pa), sa = __builtin_amdgcn_sinf(pa);
        uf[e] = pk2(ma * (p[2] * ca - p[3] * sa), -ma * (p[2] * sa + p[3] * ca));
        const float mb = __builtin_amdgcn_exp2f(p[0] * lb), pb = __builtin_amdgcn_fractf(p[1] * lb);
        vf[e] = pk2(mb * __builtin_amdgcn_cosf(pb), mb * __builtin_amdgcn_sinf(pb));
      }
      kacc = mfma32(uf, vf, kacc);
    }
    STAMP(7);
#pragma unroll
    for (int e = 0; e < 4; e++) {
      T[(e + 4 * h) + 8 * r] = kacc[e];
      const int v1 = h ? ((e < 2 ? 384 + e : 448 + e - 2) + 2 * r) : (256 + e + 4 * r);
      T[v1] = kacc[4 + e];
      if (h == 0) T[512 + 32 * e + r] = kacc[8 + e];
    }
    if (h == 1 && r == 0) T[640] = kacc[8];
  }
  gsync(smem, 2 + ch, 4);
  STAMP(19);
  {
    unsigned* c0 = (unsigned*)(smem + SS_KR + (2 * ch) * SS_KP);
    unsigned* c1 = (unsigned*)(smem + SS_KR + (2 * ch + 1) * SS_KP);
    const float* T0 = (const float*)(smem + SS_T + (2 * ch) * SS_TP);
    auto kval = [&](int l) -> float {
      const unsigned u = pk2((float)l, 0.f) & 0xffffu;
      int vi = l < 128 ? l : (int)u - 17024;
      vi = ((unsigned)l < (unsigned)L) ? vi : 641;
      return T0[vi] + T0[648 + vi];
    };
#pragma unroll 3
    for (int j = t128; j < 1052; j += 128) {
      const int l1 = 2080 - 2 * j;
      const float k0 = kval(l1), k1 = kval(l1 - 1), k2 = kval(l1 - 2);
      c0[j] = pk2(k0, k1); c1[j] = pk2(k1, k2);
    }
  }
}

DEVI void ssm_conv(const Args& a, char* smem, int b, int w4, int lane, u64 tag) {
  char* ws = a.ws;
  const int ch = w4 >> 1, hf = w4 & 1, d = 2 * b + ch, t128 = hf * 64 + lane;
  wait_flags(ws, 0, blk_of(lane & 31, b >> 5), lane < 32, tag);
  STAMP(12);
  {
    const __amdgpu_buffer_rsrc_t xr = mk_rsrc(ws + OFF_XNT, XNT_BYTES);
    const u32x4 u0 = __builtin_amdgcn_raw_buffer_load_b128(xr, d * L * 2 + t128 * 16, 0, SC1);
    const u32x4 u1 = __builtin_amdgcn_raw_buffer_load_b128(xr, d * L * 2 + 2048 + t128 * 16, 0, SC1);
    gsync(smem, 4 + 1, 4);
    char* ub = smem + SS_U + ch * SS_UP;
    *(u32x4*)(ub + 544 + t128 * 16) = u0; *(u32x4*)(ub + 544 + 2048 + t128 * 16) = u1;
    if (t128 < 34) *(u32x4*)(ub + t128 * 16) = (u32x4){0u, 0u, 0u, 0u};
  }
  gsync(smem, 2 + ch, 6);
  const int aa = lane & 15, q = lane >> 4;
  f32x4 acc[8];
#pragma unroll
  for (int e = 0; e < 8; e++) acc[e] = (f32x4){0.f, 0.f, 0.f, 0.f};
  {
    const char* kb = smem + SS_KR + (2 * ch + (aa & 1)) * SS_KP + (2088 - aa - 8 * q - (aa & 1)) * 2;
    const char* ub = smem + SS_U + ch * SS_UP + 560 + 32 * aa - 16 * q;
    auto afrag = [&](int s) -> u32x4 { const unsigned* p = (const unsigned*)(kb - 64 * s); return (u32x4){p[0], p[1], p[2], p[3]}; };
    auto bfrag = [&](int sg) -> u32x4 { return *(const u32x4*)(ub - 64 * sg); };
    if (hf == 0) {
      const u32x4 b8 = bfrag(8);
#pragma unroll
      for (int cc = 0; cc < 8; cc++) acc[cc] = mfma16(afrag(8 * (cc + 1)), b8, acc[cc]);
    }
    u32x4 af[2][8], bfr[2][8];
#pragma unroll
    for (int p = 0; p < 8; p++) { af[0][p] = afrag(8 * p + 4 * hf); bfr[0][p] = bfrag(4 * hf - 8 * p); }
#pragma unroll
    for (int ii = 0; ii < 4; ii++) {
      if (ii < 3) {
#pragma unroll
        for (int p = 0; p < 8; p++) { af[(ii + 1) & 1][p] = afrag(8 * p + 4 * hf + ii + 1); bfr[(ii + 1) & 1][p] = bfrag(4 * hf + ii + 1 - 8 * p); }
      }
#pragma unroll
      for (int cc = 0; cc < 8; cc++)
#pragma unroll
        for (int p = 0; p <= cc; p++) acc[cc] = mfma16(af[ii & 1][p], bfr[ii & 1][cc - p], acc[cc]);
    }
  }
  STAMP(13);
  gsync(smem, 2 + ch, 8);
  char* red = smem + SS_KR + (2 * ch) * SS_KP;
  if (hf == 0) {
#pragma unroll
    for (int e = 0; e < 4; e++) *(f32x4*)(red + e * 1024 + lane * 16) = acc[4 + e];
  } else {
#pragma unroll
    for (int e = 0; e < 4; e++) *(f32x4*)(red + 4096 + e * 1024 + lane * 16) = acc[e];
  }
  gsync(smem, 2 + ch, 10);
  const float dsk = bf2f(a.Dskip[d]);
  const __amdgpu_buffer_rsrc_t yr = mk_rsrc(ws + OFF_YAT, XNT_BYTES);
#pragma unroll
  for (int e = 0; e < 4; e++) {
    const f32x4 o4 = *(const f32x4*)(red + (hf ? 0 : 4096) + e * 1024 + lane * 16);
    const f32x4 alo = acc[e], ahi = acc[4 + e];
    const f32x4 mine = hf ? ahi : alo;
    const int t = 256 * (4 * hf + e) + 16 * aa + 4 * q;
    const u32x2 uv = *(const u32x2*)(smem + SS_U + ch * SS_UP + 544 + t * 2);
    const float y0 = rbf(mine[0] + o4[0]) + rbf(lo2f(uv[0]) * dsk), y1 = rbf(mine[1] + o4[1]) + rbf(hi2f(uv[0]) * dsk);
    const float y2 = rbf(mine[2] + o4[2]) + rbf(lo2f(uv[1]) * dsk), y3 = rbf(mine[3] + o4[3]) + rbf(hi2f(uv[1]) * dsk);
    __builtin_amdgcn_raw_buffer_store_b64((u32x2){pk2(y0, y1), pk2(y2, y3)}, yr, (d * L + t) * 2, 0, SC1);
  }
  WAIT_VM(0);
  gsync(smem, 4, 4);
  if (w4 == 0 && lane == 0) set_flag(ws, 2, b, tag);
  STAMP(14);
}

DEVI void phase3(const Args& a, char* smem, int rt, int dt, int w, int lane, u64 tag) {
  char* ws = a.ws;
  const int c = lane & 15, q = lane >> 4;
  char* wsl = smem + w * 28672;
  {
    const int row = lane >> 3, slot = lane & 7;
    const int chunk = (((slot >> 1) ^ ((row >> 1) & 3)) << 1) | (slot & 1);
    const unsigned vo = (unsigned)(row * D + chunk * 8) * 2u;
    const u16* src = a.w_out + (size_t)(224 * w) * D + 64 * dt;
#pragma unroll 4
    for (int k = 0; k < 28; k++) glds16u(src + (size_t)(8 * k) * D, vo, wsl + k * 1024);
  }
  const int t = 64 * rt + trow(w, c);
  u32x2 xv[4];
#pragma unroll
  for (int n = 0; n < 4; n++) xv[n] = *(const u32x2*)(a.x + (size_t)t * D + 64 * dt + 16 * n + 4 * q);
  wait_flags(ws, 2, 32 * dt + (lane & 31), lane < 32, tag);
  {
    const __amdgpu_buffer_rsrc_t yr = mk_rsrc(ws + OFF_YAT, XNT_BYTES);
#pragma unroll
    for (int e = 0; e < 2; e++) {
      const int row = 16 * w + 8 * e + (lane >> 3);
      const u32x4 yv = __builtin_amdgcn_raw_buffer_load_b128(yr, ((64 * dt + row) * L + 64 * rt) * 2 + (lane & 7) * 16, 0, SC1);
      *(u32x4*)(smem + LDS_MISC + row * 128 + (lane & 7) * 16) = yv;
    }
  }
  wait_flags(ws, 1, blk_of(rt, 2 * w + (lane & 1)), lane < 2, tag);
  STAMP(16);
  u32x4 hf[7][4];
  {
    const unsigned voff = lane * 16;
#pragma unroll
    for (int i = 0; i < 4; i++) {
      const char* hb = ws + OFF_HF + (size_t)((4 * rt + ((w + i) & 3)) * 28 + 7 * w) * 1024 + 3072;
      asm volatile("global_load_dwordx4 %0, %7, %8 offset:-3072 sc1\n\tglobal_load_dwordx4 %1, %7, %8 offset:-2048 sc1\n\t"
                   "global_load_dwordx4 %2, %7, %8 offset:-1024 sc1\n\tglobal_load_dwordx4 %3, %7, %8 sc1\n\t"
                   "global_load_dwordx4 %4, %7, %8 offset:1024 sc1\n\tglobal_load_dwordx4 %5, %7, %8 offset:2048 sc1\n\t"
                   "global_load_dwordx4 %6, %7, %8 offset:3072 sc1"
                   : "=&v"(hf[0][i]), "=&v"(hf[1][i]), "=&v"(hf[2][i]), "=&v"(hf[3][i]), "=&v"(hf[4][i]), "=&v"(hf[5][i]), "=&v"(hf[6][i])
                   : "v"(voff), "s"(hb) : "memory");
    }
#pragma unroll
    for (int s = 0; s < 7; s++) asm volatile("s_waitcnt vmcnt(0)" : "+v"(hf[s][0]), "+v"(hf[s][1]), "+v"(hf[s][2]), "+v"(hf[s][3]) :: "memory");
  }
  f32x4 acc[4][4];
#pragma unroll
  for (int i = 0; i < 4; i++)
#pragma unroll
    for (int n = 0; n < 4; n++) acc[i][n] = (f32x4){0.f, 0.f, 0.f, 0.f};
  {
    const int qq = (lane & 15) >> 2, p = lane & 3, g = ((q & 1) << 1) | (qq >> 1);
    const char* wb = wsl + (4 * q + qq) * 128 + 8 * p;
#pragma unroll
    for (int s = 0; s < 7; s++) {
      u32x4 wf[4];
#pragma unroll
      for (int n = 0; n < 4; n++) { const char* pp = wb + s * 4096 + ((n ^ g) * 32); wf[n] = ldtr(pp, pp + 2048); }
#pragma unroll
      for (int i = 0; i < 4; i++)
#pragma unroll
        for (int n = 0; n < 4; n++) acc[i][n] = mfma16(wf[n], hf[s][i], acc[i][n]);
    }
  }
  WAIT_LGKM0();
#pragma unroll
  for (int i = 1; i < 4; i++)
#pragma unroll
    for (int n = 0; n < 4; n++) *(f32x4*)(wsl + ((i - 1) * 4 + n) * 1024 + lane * 16) = acc[i][n];
  gsync(smem, 0, 12);
#pragma unroll
  for (int i = 1; i < 4; i++)
#pragma unroll
    for (int n = 0; n < 4; n++) acc[0][n] += *(const f32x4*)(smem + ((w - i) & 3) * 28672 + ((i - 1) * 4 + n) * 1024 + lane * 16);
  {
    const int qq = (lane & 15) >> 2, p = lane & 3;
#pragma unroll
    for (int n = 0; n < 4; n++) {
      const char* yp = smem + LDS_MISC + (16 * n + 4 * q + qq) * 128 + 64 * (w >> 1) + 16 * p + 8 * (w & 1);
      const u32x2 ya = __builtin_bit_cast(u32x2, __builtin_amdgcn_ds_read_tr16_b64_v4i16((LAS s16x4*)yp));
      const float o0 = rbf(lo2f(xv[n][0]) + lo2f(ya[0])) + rbf(acc[0][n][0]), o1 = rbf(hi2f(xv[n][0]) + hi2f(ya[0])) + rbf(acc[0][n][1]);
      const float o2 = rbf(lo2f(xv[n][1]) + lo2f(ya[1])) + rbf(acc[0][n][2]), o3 = rbf(hi2f(xv[n][1]) + hi2f(ya[1])) + rbf(acc[0][n][3]);
      *(u32x2*)(a.out + (size_t)t * D + 64 * dt + 16 * n + 4 * q) = (u32x2){pk2(o0, o1), pk2(o2, o3)};
    }
  }
  STAMP(17);
}

#define SPLIT 0
__global__ __launch_bounds__(512, 2) void fused_kernel(Args a, int phases) {
  extern __shared__ __attribute__((aligned(16))) char smem[];
  char* ws = a.ws;
  const int b = blockIdx.x, xcd = b & 7, i0 = b >> 3, rt = 8 * (xcd >> 1) + (i0 & 7), ct = 4 * (xcd & 1) + (i0 >> 3);
  const int lane = threadIdx.x & 63, w = __builtin_amdgcn_readfirstlane(threadIdx.x >> 6);
  STAMP(0);
  const u64 tag = *(const u64*)(ws + OFF_NONCE) + 1;
  if (threadIdx.x < 8) ((unsigned*)(smem + LDS_SYNC))[threadIdx.x] = 0u;
  if (!SPLIT || (phases & 1)) phase1(a, smem, rt, ct, w, lane, tag);
  if (w < 4) {
    if (!SPLIT || (phases & 2)) {
      phase2_gemm(a, smem, rt, ct, w, lane, tag);
    }
    if (!SPLIT || (phases & 4)) {
      if (SPLIT && !(phases & 2)) { if (lane == 0) atomicAdd((unsigned*)(smem + LDS_SYNC), 2u); }
      phase3(a, smem, rt, ct, w, lane, tag);
    }
  } else {
    if (!SPLIT || (phases & 2)) {
      ssm_kgen(a, smem, b, w - 4, lane);
      STAMP(11);
      ssm_conv(a, smem, b, w - 4, lane, tag);
    }
    if (b == 0 && w == 4 && (!SPLIT || (phases & 4))) {
#pragma unroll 1
      for (int k = 0; k < 4; k++) wait_flags(ws, 0, k * 64 + lane, true, tag);
      if (lane == 0) __hip_atomic_store((gu64*)(ws + OFF_NONCE), tag, RLX_AGENT);
    }
  }
}

extern "C" void kernel_launch(void* const* d_in, const int* in_sizes, int n_in, void* d_out, int out_size, void* d_ws, size_t ws_size,
                              hipStream_t stream) {
  Args a;
  a.x = (const u16*)d_in[0];
  a.Are = (const u16*)d_in[1]; a.Aim = (const u16*)d_in[2]; a.Bre = (const u16*)d_in[3]; a.Bim = (const u16*)d_in[4];
  a.Cre = (const u16*)d_in[5]; a.Cim = (const u16*)d_in[6]; a.logdt = (const u16*)d_in[7]; a.Dskip = (const u16*)d_in[8];
  a.w_in = (const u16*)d_in[10]; a.w_sp = (const u16*)d_in[11]; a.b_sp = (const u16*)d_in[12]; a.w_out = (const u16*)d_in[13];
  a.out = (u16*)d_out; a.ws = (char*)d_ws;
  (void)hipFuncSetAttribute((const void*)fused_kernel, hipFuncAttributeMaxDynamicSharedMemorySize, LDS_TOTAL);
  fused_kernel<<<256, 512, LDS_TOTAL, stream>>>(a, 7);
}
```
